```python
import torch

def _softmax(x, axis=-1):
    return torch.softmax(x, dim=axis)
def _gelu(x):
    return torch.nn.functional.gelu(x, approximate="tanh")
def _silu(x):
    return torch.nn.functional.silu(x)
def _sigmoid(x):
    return torch.sigmoid(x)
def _layernorm(x, axis=-1, eps=1e-5):
    m = x.mean(dim=axis, keepdim=True)
    v = x.var(dim=axis, keepdim=True, unbiased=False)
    return (x - m) / torch.sqrt(v + eps)
def _scatter_add(tgt, idx, src, axis=0):
    return tgt.clone().index_add_(axis, idx, src)
def _segment_sum(src, seg, n):
    out = torch.zeros((n,) + src.shape[1:], dtype=src.dtype)
    return out.index_add_(0, seg, src)
def _pad(x, pw):
    flat = tuple(v for pair in reversed(pw) for v in pair)
    return torch.nn.functional.pad(x, flat)

kernel_name = 'parallel_sum__mha_block__glu_gated_geglu__prompt'

def setup_inputs():

    _w = (lambda a: (lambda t: t.to(torch.bfloat16) if t.is_floating_point() else t)(torch.as_tensor(a)))
    def _A_setup():
        B, S, D, H = 1, 2048, 640, 32
        return {
            "x": _w((lambda *s: torch.randn(*s, dtype=torch.bfloat16) * 0.05)(B, S, D)),
            "wqkv": _w((lambda *s: torch.randn(*s, dtype=torch.bfloat16) * 0.05)(D, 3 * D)),
            "wo": _w((lambda *s: torch.randn(*s, dtype=torch.bfloat16) * 0.05)(D, D)),
        }
    _A_in = _A_setup()

    def _B_setup():
        B, S, Dm, Df = 1, 2048, 640, 1728
        return {
            "x": _w((lambda *s: torch.randn(*s, dtype=torch.bfloat16) * 0.05)(B, S, Dm)),
            "w_gate": _w((lambda *s: torch.randn(*s, dtype=torch.bfloat16) * 0.8)(Dm, Df) * (Dm ** -0.5)),
            "w_up": _w((lambda *s: torch.randn(*s, dtype=torch.bfloat16) * 0.8)(Dm, Df) * (Dm ** -0.5)),
            "w_down": _w((lambda *s: torch.randn(*s, dtype=torch.bfloat16) * 0.8)(Df, Dm) * (Df ** -0.5)),
        }
    _B_in = _B_setup()

    return {
        "A_x": _A_in["x"],
        "A_wqkv": _A_in["wqkv"],
        "A_wo": _A_in["wo"],
        "B_x": _B_in["x"],
        "B_w_gate": _B_in["w_gate"],
        "B_w_up": _B_in["w_up"],
        "B_w_down": _B_in["w_down"],
    }

def reference(A_x, A_wqkv, A_wo, B_x, B_w_gate, B_w_up, B_w_down):
    def _A_ref(x, wqkv, wo):
        B, S, D = x.shape
        H = 32
        Dh = D // H
        qkv = (lambda x, *d: x.permute(*d))((x @ wqkv).reshape(B, S, 3, H, Dh), 2, 0, 3, 1, 4)
        q, k, v = qkv[0], qkv[1], qkv[2]
        scores = torch.einsum("bhqd,bhkd->bhqk", q, k) * (Dh ** -0.5)
        p = _softmax(scores, axis=-1)
        o = torch.einsum("bhqk,bhkd->bhqd", p, v)
        return (lambda x, *d: x.permute(*d))(o, 0, 2, 1, 3).reshape(B, S, D) @ wo

    def _B_ref(x, w_gate, w_up, w_down):
        g = torch.einsum("bsd,df->bsf", x, w_gate)
        u = torch.einsum("bsd,df->bsf", x, w_up)
        h = _gelu(g) * u
        return torch.einsum("bsf,fd->bsd", h, w_down)

    x = A_x
    xn = _layernorm(x, axis=-1)
    return x + _A_ref(xn, A_wqkv, A_wo) + _B_ref(xn, B_w_gate, B_w_up, B_w_down)
```

```cpp
#include <hip/hip_runtime.h>
#include <stdint.h>

typedef __attribute__((ext_vector_type(8))) short bf16x8;
typedef __attribute__((ext_vector_type(4))) short s16x4;
typedef __attribute__((ext_vector_type(4))) float f32x4;
typedef __attribute__((ext_vector_type(2))) float f32x2;
typedef __attribute__((ext_vector_type(16))) float f32x16;
typedef __attribute__((ext_vector_type(2))) __bf16 bf2_t;
typedef __attribute__((ext_vector_type(4))) unsigned u32x4;
typedef __attribute__((ext_vector_type(2))) unsigned u32x2;
typedef unsigned short u16;
typedef __attribute__((address_space(3))) s16x4 lds_s16x4;

#define DI __device__ __forceinline__

constexpr int SEQ = 2048, DM = 640, NH = 32, DH = 20, DF = 1728;

DI unsigned pack_bf2(float a, float b) {
  f32x2 v = {a, b};
  return __builtin_bit_cast(unsigned, __builtin_convertvector(v, bf2_t));
}
DI float bf_lo(unsigned u) { return __uint_as_float(u << 16); }
DI float bf_hi(unsigned u) { return __uint_as_float(u & 0xffff0000u); }
DI float rbf(float x) { return bf_lo(pack_bf2(x, 0.f)); }

DI void glds16(const void* g, char* lds) {
  __builtin_amdgcn_global_load_lds((const unsigned*)g, (unsigned*)lds, 16, 0, 0);
}
DI s16x4 tr_read(const char* lds) {
  return __builtin_amdgcn_ds_read_tr16_b64_v4i16((lds_s16x4*)lds);
}

__global__ __launch_bounds__(256) void ln_kernel(const u16* __restrict__ x, u16* __restrict__ xn) {
  const int row = blockIdx.x * 4 + (threadIdx.x >> 6), lane = threadIdx.x & 63;
  const unsigned* xr = (const unsigned*)(x + row * DM);
  float f[10];
#pragma unroll
  for (int j = 0; j < 5; ++j) {
    unsigned v = xr[lane + 64 * j];
    f[2 * j] = bf_lo(v);
    f[2 * j + 1] = bf_hi(v);
  }
  float s = 0.f;
#pragma unroll
  for (int j = 0; j < 10; ++j) s += f[j];
#pragma unroll
  for (int o = 32; o > 0; o >>= 1) s += __shfl_xor(s, o);
  const float mean = s * (1.f / DM);
  float q = 0.f;
#pragma unroll
  for (int j = 0; j < 10; ++j) { float d = f[j] - mean; q += d * d; }
#pragma unroll
  for (int o = 32; o > 0; o >>= 1) q += __shfl_xor(q, o);
  const float mb = rbf(mean);
  const float vb = rbf(q * (1.f / DM));
  const float den = rbf(sqrtf(rbf(vb + 1e-5f)));
  unsigned* o = (unsigned*)(xn + row * DM);
#pragma unroll
  for (int j = 0; j < 5; ++j)
    o[lane + 64 * j] = pack_bf2(rbf(f[2 * j] - mb) / den, rbf(f[2 * j + 1] - mb) / den);
}

constexpr int G1_STAGE = 40960;
__global__ __launch_bounds__(256, 2) void gemm1_kernel(const u16* __restrict__ xn, const u16* __restrict__ wqkv,
                                                       const u16* __restrict__ wg, const u16* __restrict__ wu,
                                                       u16* __restrict__ Qg, u16* __restrict__ Kf,
                                                       u16* __restrict__ Vf, u16* __restrict__ hbuf) {
  __shared__ __attribute__((aligned(16))) char smem[2 * G1_STAGE];
  const int b = blockIdx.x;
  const int t = (b & 7) * 56 + (b >> 3);
  const int nblk = t >> 4, mblk = t & 15;
  const int tid = threadIdx.x, wave = tid >> 6, lane = tid & 63;
  const int wm = wave >> 1, wn = wave & 1;
  const int m0 = mblk * 128;
  const bool isqkv = nblk < 10;

  const u16* a_src[4];
#pragma unroll
  for (int i = 0; i < 4; ++i) {
    int row = wave * 32 + i * 8 + (lane >> 3);
    int c = (lane & 7) ^ ((row >> 1) & 7);
    a_src[i] = xn + (size_t)(m0 + row) * DM + c * 8;
  }
  const int ldb = isqkv ? 3 * DM : DF;
  const u16* b_src[6];
#pragma unroll
  for (int i = 0; i < 6; ++i) {
    int kb = wave * 2 + i / 3, grp = i % 3;
    int nt = grp * 4 + (lane >> 4);
    int rr = ((lane >> 1) & 7) ^ ((kb & 1) << 2);
    int k = kb * 8 + rr;
    const u16* base;
    if (isqkv) base = wqkv + nblk * 192 + nt * 16;
    else base = (nt < 6) ? (wg + (nblk - 10) * 96 + nt * 16) : (wu + (nblk - 10) * 96 + (nt - 6) * 16);
    b_src[i] = base + (size_t)k * ldb + (lane & 1) * 8;
  }
  auto stage = [&](int buf, int kt) {
    char* sa = smem + buf * G1_STAGE;
    char* sb = sa + 16384;
#pragma unroll
    for (int i = 0; i < 4; ++i) glds16(a_src[i] + kt * 64, sa + (wave * 4 + i) * 1024 + lane * 16);
#pragma unroll
    for (int i = 0; i < 6; ++i) glds16(b_src[i] + (size_t)kt * 64 * ldb, sb + (wave * 6 + i) * 1024 + lane * 16);
  };

  int a_off[4];
#pragma unroll
  for (int i = 0; i < 4; ++i) {
    int row = wm * 64 + i * 16 + (lane & 15);
    a_off[i] = row * 128 + ((((lane >> 4)) ^ ((row >> 1) & 7)) << 4);
  }
  int b_off[6];
  {
    int g = lane >> 4, q = (lane & 15) >> 2, p = lane & 3;
#pragma unroll
    for (int j = 0; j < 6; ++j) {
      int nt = isqkv ? (wn * 6 + j) : (j < 3 ? wn * 3 + j : 3 + wn * 3 + j);
      b_off[j] = 16384 + g * 3072 + nt * 256 + ((g & 1) << 7) + q * 32 + p * 8;
    }
  }

  f32x4 acc[4][6];
#pragma unroll
  for (int i = 0; i < 4; ++i)
#pragma unroll
    for (int j = 0; j < 6; ++j) acc[i][j] = (f32x4){0.f, 0.f, 0.f, 0.f};

  stage(0, 0);
  for (int kt = 0; kt < 10; ++kt) {
    __syncthreads();
    if (kt + 1 < 10) stage((kt + 1) & 1, kt + 1);
    const char* sbase = smem + (kt & 1) * G1_STAGE;
#pragma unroll
    for (int kk = 0; kk < 2; ++kk) {
      bf16x8 af[4], bfr[6];
#pragma unroll
      for (int i = 0; i < 4; ++i) af[i] = *(const bf16x8*)(sbase + (a_off[i] ^ (kk << 6)));
#pragma unroll
      for (int j = 0; j < 6; ++j) {
        s16x4 lo = tr_read(sbase + b_off[j] + kk * 12288);
        s16x4 hi = tr_read(sbase + (b_off[j] ^ 128) + kk * 12288);
        bfr[j] = __builtin_shufflevector(lo, hi, 0, 1, 2, 3, 4, 5, 6, 7);
      }
#pragma unroll
      for (int i = 0; i < 4; ++i)
#pragma unroll
        for (int j = 0; j < 6; ++j)
          acc[i][j] = __builtin_amdgcn_mfma_f32_16x16x32_bf16(bfr[j], af[i], acc[i][j], 0, 0, 0);
    }
  }

  const int ln4 = (lane >> 4) * 4;
  if (isqkv) {
    const float qscale = 0.22360679774997896f * 1.4426950408889634f;
#pragma unroll
    for (int j = 0; j < 6; ++j) {
      const int n = nblk * 192 + (wn * 6 + j) * 16 + ln4;
      const int sec = n / DM, rr = n - sec * DM, head = rr / DH, d = rr - head * DH, d4 = d >> 2;
#pragma unroll
      for (int i = 0; i < 4; ++i) {
        const int tok = m0 + wm * 64 + i * 16 + (lane & 15);
        f32x4 v = acc[i][j];
        if (sec == 0) {
          u32x2 o = {pack_bf2(v[0] * qscale, v[1] * qscale), pack_bf2(v[2] * qscale, v[3] * qscale)};
          *(u32x2*)(Qg + ((size_t)(head * SEQ + tok) * 32 + d)) = o;
        } else if (sec == 1) {
          char* kb = (char*)Kf + (size_t)(head * 64 + (tok >> 5)) * 1536;
          const int r = tok & 31;
          unsigned p0 = pack_bf2(v[0], v[1]), p1 = pack_bf2(v[2], v[3]);
          if (d4 < 4) {
            u32x2 o = {p0, p1};
            *(u32x2*)(kb + ((d4 >> 1) * 32 + r) * 16 + (d4 & 1) * 8) = o;
          } else {
            u32x4 o = {p0, p1, 0u, 0u};
            *(u32x4*)(kb + 1024 + r * 16) = o;
          }
        } else {
          const int s = tok >> 4, qd = (tok >> 2) & 3;
          u16* vb = Vf + ((size_t)(head * 128 + s) * 48 + (qd & 1) * 24 + d) * 8 + (qd >> 1) * 4 + (tok & 3);
          unsigned p0 = pack_bf2(v[0], v[1]), p1 = pack_bf2(v[2], v[3]);
          vb[0] = (u16)p0;
          vb[8] = (u16)(p0 >> 16);
          vb[16] = (u16)p1;
          vb[24] = (u16)(p1 >> 16);
          if (d4 == 4) vb[32] = 0x3F80;
        }
      }
    }
  } else {
#pragma unroll
    for (int j = 0; j < 3; ++j) {
      const int f = (nblk - 10) * 96 + (wn * 3 + j) * 16 + ln4;
#pragma unroll
      for (int i = 0; i < 4; ++i) {
        const int tok = m0 + wm * 64 + i * 16 + (lane & 15);
        float hv[4];
#pragma unroll
        for (int r = 0; r < 4; ++r) {
          float g = rbf(acc[i][j][r]), u = rbf(acc[i][j + 3][r]);
          float y = 0.7978845608028654f * (g + 0.044715f * g * g * g);
          float e = __builtin_amdgcn_exp2f(-2.8853900817779268f * y);
          float ge = rbf(g * __builtin_amdgcn_rcpf(1.f + e));
          hv[r] = ge * u;
        }
        u32x2 o = {pack_bf2(hv[0], hv[1]), pack_bf2(hv[2], hv[3])};
        *(u32x2*)(hbuf + (size_t)tok * DF + f) = o;
      }
    }
  }
}

constexpr int AT_STAGE = 24576;
constexpr float AT_THR = 10.f;
__global__ __launch_bounds__(512, 2) void attn_kernel(const u16* __restrict__ Qg, const u16* __restrict__ Kf,
                                                      const u16* __restrict__ Vf, u16* __restrict__ obuf) {
  __shared__ __attribute__((aligned(16))) char smem[2 * AT_STAGE];
  const int b = blockIdx.x;
  const int head = (b & 7) * 4 + ((b >> 3) & 3), qb = b >> 5;
  const int tid = threadIdx.x, wave = tid >> 6, lane = tid & 63;
  const int r = lane & 31, h = lane >> 5;
  const int q = qb * 256 + wave * 32 + r;

  const char* kg = (const char*)Kf + (size_t)head * 98304;
  const char* vg = (const char*)Vf + (size_t)head * 98304;
  auto stage = [&](int buf, int tile) {
#pragma unroll
    for (int i = 0; i < 3; ++i) {
      int idx = wave * 3 + i;
      const char* src = (idx < 12) ? (kg + tile * 12288 + idx * 1024) : (vg + tile * 12288 + (idx - 12) * 1024);
      glds16(src + lane * 16, smem + buf * AT_STAGE + idx * 1024 + lane * 16);
    }
  };
  stage(0, 0);

  const u16* qrow = Qg + (size_t)(head * SEQ + q) * 32;
  bf16x8 q1 = *(const bf16x8*)(qrow + 8 * h);
  bf16x8 q2 = {0, 0, 0, 0, 0, 0, 0, 0};
  {
    s16x4 t4 = *(const s16x4*)(qrow + 16);
    if (h == 0) { q2[0] = t4[0]; q2[1] = t4[1]; q2[2] = t4[2]; q2[3] = t4[3]; }
  }

  const int k1_off = lane * 16, k2_off = 1024 + r * 16;
  const int v_off = 12288 + (h * 24 + (r < 23 ? r : 23)) * 16;

  f32x16 o, negm;
#pragma unroll
  for (int i = 0; i < 16; ++i) { o[i] = 0.f; negm[i] = 0.f; }

  for (int tile = 0; tile < 8; ++tile) {
    __syncthreads();
    if (tile + 1 < 8) stage((tile + 1) & 1, tile + 1);
    const char* sb = smem + (tile & 1) * AT_STAGE;
    if (tile == 0) {
      f32x16 z;
#pragma unroll
      for (int i = 0; i < 16; ++i) z[i] = 0.f;
      f32x16 s0 = __builtin_amdgcn_mfma_f32_32x32x16_bf16(*(const bf16x8*)(sb + k1_off), q1, z, 0, 0, 0);
      s0 = __builtin_amdgcn_mfma_f32_32x32x16_bf16(*(const bf16x8*)(sb + k2_off), q2, s0, 0, 0, 0);
      float mx = s0[0];
#pragma unroll
      for (int i = 1; i < 16; ++i) mx = fmaxf(mx, s0[i]);
      mx = fmaxf(mx, __shfl_xor(mx, 32));
#pragma unroll
      for (int i = 0; i < 16; ++i) negm[i] = -mx;
    }
#pragma unroll
    for (int g = 0; g < 4; ++g) {
      const char* kb = sb + g * 3072;
      f32x16 s0 = __builtin_amdgcn_mfma_f32_32x32x16_bf16(*(const bf16x8*)(kb + k1_off), q1, negm, 0, 0, 0);
      s0 = __builtin_amdgcn_mfma_f32_32x32x16_bf16(*(const bf16x8*)(kb + k2_off), q2, s0, 0, 0, 0);
      f32x16 s1 = __builtin_amdgcn_mfma_f32_32x32x16_bf16(*(const bf16x8*)(kb + 1536 + k1_off), q1, negm, 0, 0, 0);
      s1 = __builtin_amdgcn_mfma_f32_32x32x16_bf16(*(const bf16x8*)(kb + 1536 + k2_off), q2, s1, 0, 0, 0);
      float mx = fmaxf(s0[0], s1[0]);
#pragma unroll
      for (int i = 1; i < 16; ++i) mx = fmaxf(mx, fmaxf(s0[i], s1[i]));
      if (__builtin_amdgcn_ballot_w64(mx > AT_THR) != 0) {
        mx = fmaxf(mx, __shfl_xor(mx, 32));
        const float delta = fmaxf(mx, 0.f);
        const float alpha = __builtin_amdgcn_exp2f(-delta);
#pragma unroll
        for (int i = 0; i < 16; ++i) { negm[i] -= delta; s0[i] -= delta; s1[i] -= delta; o[i] *= alpha; }
      }
      unsigned p[16];
#pragma unroll
      for (int i = 0; i < 8; ++i) {
        p[i] = pack_bf2(__builtin_amdgcn_exp2f(s0[2 * i]), __builtin_amdgcn_exp2f(s0[2 * i + 1]));
        p[8 + i] = pack_bf2(__builtin_amdgcn_exp2f(s1[2 * i]), __builtin_amdgcn_exp2f(s1[2 * i + 1]));
      }
#pragma unroll
      for (int s = 0; s < 4; ++s) {
        u32x4 pv = {p[4 * s], p[4 * s + 1], p[4 * s + 2], p[4 * s + 3]};
        bf16x8 vf = *(const bf16x8*)(sb + (g * 4 + s) * 768 + v_off);
        o = __builtin_amdgcn_mfma_f32_32x32x16_bf16(vf, __builtin_bit_cast(bf16x8, pv), o, 0, 0, 0);
      }
    }
  }
  const float l = __shfl(o[8], r + 32);
  const float inv = 1.f / l;
  u16* orow = obuf + (size_t)q * DM + head * DH + 4 * h;
  {
    u32x2 w0 = {pack_bf2(o[0] * inv, o[1] * inv), pack_bf2(o[2] * inv, o[3] * inv)};
    u32x2 w1 = {pack_bf2(o[4] * inv, o[5] * inv), pack_bf2(o[6] * inv, o[7] * inv)};
    *(u32x2*)(orow) = w0;
    *(u32x2*)(orow + 8) = w1;
    if (h == 0) {
      u32x2 w2 = {pack_bf2(o[8] * inv, o[9] * inv), pack_bf2(o[10] * inv, o[11] * inv)};
      *(u32x2*)(orow + 16) = w2;
    }
  }
}

constexpr int G2_STAGE = 36864;
__global__ __launch_bounds__(256, 2) void gemm2_kernel(const u16* __restrict__ obuf, const u16* __restrict__ hbuf,
                                                       const u16* __restrict__ wo, const u16* __restrict__ wd,
                                                       const u16* __restrict__ x, u16* __restrict__ out) {
  __shared__ __attribute__((aligned(16))) char smem[81920];
  const int b = blockIdx.x;
  const int nblk = b & 7, mblk = b >> 3;
  const int tid = threadIdx.x, wave = tid >> 6, lane = tid & 63;
  const int m0 = mblk * 64, n0 = nblk * 80;

  int a_row[4], a_c[4];
#pragma unroll
  for (int i = 0; i < 4; ++i) {
    int row = (wave * 4 + i) * 4 + (lane >> 4);
    a_row[i] = m0 + row;
    a_c[i] = ((lane & 15) ^ (row & 15)) * 8;
  }
  int b_o[5];
#pragma unroll
  for (int i = 0; i < 5; ++i) {
    int sid = (wave * 5 + i) * 4 + (lane >> 4);
    int kb = sid / 5, nt = sid - kb * 5;
    int rr = ((lane >> 1) & 7) ^ ((kb & 1) << 2);
    b_o[i] = (kb * 8 + rr) * DM + n0 + nt * 16 + (lane & 1) * 8;
  }
  auto stage = [&](int buf, int st) {
    char* sa = smem + buf * G2_STAGE;
    char* sb = sa + 16384;
    const u16* A; const u16* B; int lda, k0;
    if (st < 5) { A = obuf; lda = DM; k0 = st * 128; B = wo; }
    else { A = hbuf; lda = DF; k0 = (st - 5) * 128; if (k0 > DF - 128) k0 = DF - 128; B = wd; }
#pragma unroll
    for (int i = 0; i < 4; ++i) glds16(A + (size_t)a_row[i] * lda + k0 + a_c[i], sa + (wave * 4 + i) * 1024 + lane * 16);
#pragma unroll
    for (int i = 0; i < 5; ++i) glds16(B + (size_t)k0 * DM + b_o[i], sb + (wave * 5 + i) * 1024 + lane * 16);
  };

  int a_off[4];
#pragma unroll
  for (int i = 0; i < 4; ++i) {
    int row = i * 16 + (lane & 15);
    a_off[i] = row * 256 + (((wave * 4 + (lane >> 4)) ^ (row & 15)) << 4);
  }
  int b_off;
  {
    int g = lane >> 4, q = (lane & 15) >> 2, p = lane & 3;
    b_off = 16384 + (wave * 4 + g) * 1280 + ((g & 1) << 7) + q * 32 + p * 8;
  }
  f32x4 acc[4][5];
#pragma unroll
  for (int i = 0; i < 4; ++i)
#pragma unroll
    for (int j = 0; j < 5; ++j) acc[i][j] = (f32x4){0.f, 0.f, 0.f, 0.f};

  constexpr int NST = 5 + 14;
  stage(0, 0);
  for (int st = 0; st < NST; ++st) {
    __syncthreads();
    if (st + 1 < NST) stage((st + 1) & 1, st + 1);
    const char* sbase = smem + (st & 1) * G2_STAGE;
    if (st == NST - 1 && wave < 2) continue;
    bf16x8 af[4], bfr[5];
#pragma unroll
    for (int i = 0; i < 4; ++i) af[i] = *(const bf16x8*)(sbase + a_off[i]);
#pragma unroll
    for (int j = 0; j < 5; ++j) {
      s16x4 lo = tr_read(sbase + b_off + j * 256);
      s16x4 hi = tr_read(sbase + (b_off ^ 128) + j * 256);
      bfr[j] = __builtin_shufflevector(lo, hi, 0, 1, 2, 3, 4, 5, 6, 7);
    }
#pragma unroll
    for (int i = 0; i < 4; ++i)
#pragma unroll
      for (int j = 0; j < 5; ++j)
        acc[i][j] = __builtin_amdgcn_mfma_f32_16x16x32_bf16(bfr[j], af[i], acc[i][j], 0, 0, 0);
  }
  __syncthreads();
  float* red = (float*)smem;
#pragma unroll
  for (int i = 0; i < 4; ++i)
#pragma unroll
    for (int j = 0; j < 5; ++j)
      *(f32x4*)(red + (wave * 64 + i * 16 + (lane & 15)) * 80 + j * 16 + (lane >> 4) * 4) = acc[i][j];
  __syncthreads();
#pragma unroll
  for (int i = 0; i < 5; ++i) {
    int item = tid + 256 * i;
    int row = item / 20, c4 = item - row * 20;
    f32x4 s = *(const f32x4*)(red + row * 80 + c4 * 4);
#pragma unroll
    for (int w = 1; w < 4; ++w) s += *(const f32x4*)(red + (w * 64 + row) * 80 + c4 * 4);
    size_t gi = (size_t)(m0 + row) * DM + n0 + c4 * 4;
    u32x2 xv = *(const u32x2*)(x + gi);
    u32x2 o = {pack_bf2(s[0] + bf_lo(xv[0]), s[1] + bf_hi(xv[0])), pack_bf2(s[2] + bf_lo(xv[1]), s[3] + bf_hi(xv[1]))};
    *(u32x2*)(out + gi) = o;
  }
}

extern "C" void kernel_launch(void* const* d_in, const int* in_sizes, int n_in, void* d_out, int out_size,
                              void* d_ws, size_t ws_size, hipStream_t stream) {
  const u16* x = (const u16*)d_in[0];
  const u16* wqkv = (const u16*)d_in[1];
  const u16* wo = (const u16*)d_in[2];
  const u16* wg = (const u16*)d_in[4];
  const u16* wu = (const u16*)d_in[5];
  const u16* wd = (const u16*)d_in[6];
  char* ws = (char*)d_ws;
  u16* xn = (u16*)(ws);
  u16* Qg = (u16*)(ws + (4u << 20));
  u16* Kf = (u16*)(ws + (8u << 20));
  u16* Vf = (u16*)(ws + (12u << 20));
  u16* hb = (u16*)(ws + (16u << 20));
  u16* ob = (u16*)(ws + (24u << 20));
  ln_kernel<<<SEQ / 4, 256, 0, stream>>>(x, xn);
  gemm1_kernel<<<448, 256, 0, stream>>>(xn, wqkv, wg, wu, Qg, Kf, Vf, hb);
  attn_kernel<<<256, 512, 0, stream>>>(Qg, Kf, Vf, ob);
  gemm2_kernel<<<256, 256, 0, stream>>>(ob, hb, wo, wd, x, (u16*)d_out);
}
```

```cpp
#include <hip/hip_runtime.h>
#include <stdint.h>

typedef __attribute__((ext_vector_type(8))) short bf16x8;
typedef __attribute__((ext_vector_type(4))) short s16x4;
typedef __attribute__((ext_vector_type(4))) float f32x4;
typedef __attribute__((ext_vector_type(2))) float f32x2;
typedef __attribute__((ext_vector_type(16))) float f32x16;
typedef __attribute__((ext_vector_type(2))) __bf16 bf2_t;
typedef __attribute__((ext_vector_type(4))) unsigned u32x4;
typedef __attribute__((ext_vector_type(2))) unsigned u32x2;
typedef unsigned short u16;
typedef __attribute__((address_space(3))) s16x4 lds_s16x4;

#define DI __device__ __forceinline__

constexpr int SEQ = 2048, DM = 640, NH = 32, DH = 20, DF = 1728;

DI unsigned pack_bf2(float a, float b) {
  f32x2 v = {a, b};
  return __builtin_bit_cast(unsigned, __builtin_convertvector(v, bf2_t));
}
DI float bf_lo(unsigned u) { return __uint_as_float(u << 16); }
DI float bf_hi(unsigned u) { return __uint_as_float(u & 0xffff0000u); }
DI float rbf(float x) { return bf_lo(pack_bf2(x, 0.f)); }

DI void glds16(const void* g, unsigned lds_addr) {
  unsigned keep;
  asm volatile("s_mov_b32 %0, m0\n\ts_mov_b32 m0, %2\n\ts_nop 0\n\tglobal_load_lds_dwordx4 %1, off\n\ts_mov_b32 m0, %0"
               : "=&s"(keep) : "v"(g), "s"(lds_addr) : "memory");
}
DI void wait_vm0() { asm volatile("s_waitcnt vmcnt(0)" ::: "memory"); }
DI void barrier_raw() {
  asm volatile("s_waitcnt lgkmcnt(0)" ::: "memory");
  __builtin_amdgcn_s_barrier();
  asm volatile("" ::: "memory");
}
DI int imax3(int a, int b, int c) { return max(a, max(b, c)); }
DI s16x4 tr_read(const char* lds) {
  return __builtin_amdgcn_ds_read_tr16_b64_v4i16((lds_s16x4*)lds);
}

__global__ __launch_bounds__(256) void ln_kernel(const u16* __restrict__ x, u16* __restrict__ xn) {
  const int row = blockIdx.x * 4 + (threadIdx.x >> 6), lane = threadIdx.x & 63;
  const unsigned* xr = (const unsigned*)(x + row * DM);
  float f[10];
#pragma unroll
  for (int j = 0; j < 5; ++j) {
    unsigned v = xr[lane + 64 * j];
    f[2 * j] = bf_lo(v);
    f[2 * j + 1] = bf_hi(v);
  }
  float s = 0.f;
#pragma unroll
  for (int j = 0; j < 10; ++j) s += f[j];
#pragma unroll
  for (int o = 32; o > 0; o >>= 1) s += __shfl_xor(s, o);
  const float mean = s * (1.f / DM);
  float q = 0.f;
#pragma unroll
  for (int j = 0; j < 10; ++j) { float d = f[j] - mean; q += d * d; }
#pragma unroll
  for (int o = 32; o > 0; o >>= 1) q += __shfl_xor(q, o);
  const float mb = rbf(mean);
  const float vb = rbf(q * (1.f / DM));
  const float den = rbf(sqrtf(rbf(vb + 1e-5f)));
  unsigned* o = (unsigned*)(xn + row * DM);
#pragma unroll
  for (int j = 0; j < 5; ++j)
    o[lane + 64 * j] = pack_bf2(rbf(f[2 * j] - mb) / den, rbf(f[2 * j + 1] - mb) / den);
}

constexpr int G1_STAGE = 57344;
__global__ __launch_bounds__(512, 2) void gemm1_kernel(const u16* __restrict__ xn, const u16* __restrict__ wqkv,
                                                       const u16* __restrict__ wg, const u16* __restrict__ wu,
                                                       u16* __restrict__ Qg, u16* __restrict__ Kf,
                                                       u16* __restrict__ Vf, u16* __restrict__ hbuf) {
  __shared__ __attribute__((aligned(16))) char smem[2 * G1_STAGE];
  const int b = blockIdx.x;
  const int t = (b & 7) * 28 + (b >> 3);
  const int nblk = t >> 3, mblk = t & 7;
  const int tid = threadIdx.x, wave = tid >> 6, lane = tid & 63;
  const int wm = wave >> 1, wn = wave & 1;
  const int m0 = mblk * 256;
  const bool isqkv = nblk < 10;

  const u16* a_src[4];
#pragma unroll
  for (int i = 0; i < 4; ++i) {
    int row = wave * 32 + i * 8 + (lane >> 3);
    int c = (lane & 7) ^ ((row >> 1) & 7);
    a_src[i] = xn + (size_t)(m0 + row) * DM + c * 8;
  }
  const int ldb = isqkv ? 3 * DM : DF;
  const u16* b_src[3];
#pragma unroll
  for (int i = 0; i < 3; ++i) {
    int kb = wave;
    int nt = i * 4 + (lane >> 4);
    int rr = ((lane >> 1) & 7) ^ ((kb & 1) << 2);
    int k = kb * 8 + rr;
    const u16* base;
    if (isqkv) base = wqkv + nblk * 192 + nt * 16;
    else base = (nt < 6) ? (wg + (nblk - 10) * 96 + nt * 16) : (wu + (nblk - 10) * 96 + (nt - 6) * 16);
    b_src[i] = base + (size_t)k * ldb + (lane & 1) * 8;
  }
  const unsigned lds0 = __builtin_amdgcn_readfirstlane((unsigned)(size_t)smem);
  const unsigned wv = __builtin_amdgcn_readfirstlane(wave);
  auto stage = [&](int buf, int kt) {
    unsigned sa = lds0 + buf * G1_STAGE;
    unsigned sb = sa + 32768;
#pragma unroll
    for (int i = 0; i < 4; ++i) glds16(a_src[i] + kt * 64, sa + (wv * 4 + i) * 1024);
#pragma unroll
    for (int i = 0; i < 3; ++i) glds16(b_src[i] + (size_t)kt * 64 * ldb, sb + (wv * 3 + i) * 1024);
  };
  stage(0, 0);
  stage(1, 1);

  int a_off[4];
#pragma unroll
  for (int i = 0; i < 4; ++i) {
    int row = wm * 64 + i * 16 + (lane & 15);
    a_off[i] = row * 128 + ((((lane >> 4)) ^ ((row >> 1) & 7)) << 4);
  }
  int b_off[6];
  {
    int g = lane >> 4, q = (lane & 15) >> 2, p = lane & 3;
#pragma unroll
    for (int j = 0; j < 6; ++j) {
      int nt = isqkv ? (wn * 6 + j) : (j < 3 ? wn * 3 + j : 3 + wn * 3 + j);
      b_off[j] = 32768 + g * 3072 + nt * 256 + ((g & 1) << 7) + q * 32 + p * 8;
    }
  }

  f32x4 acc[4][6];
#pragma unroll
  for (int i = 0; i < 4; ++i)
#pragma unroll
    for (int j = 0; j < 6; ++j) acc[i][j] = (f32x4){0.f, 0.f, 0.f, 0.f};

  for (int kt = 0; kt < 10; ++kt) {
    if (kt == 0) asm volatile("s_waitcnt vmcnt(7)" ::: "memory");
    else wait_vm0();
    barrier_raw();
    if (kt >= 1 && kt + 1 < 10) stage((kt + 1) & 1, kt + 1);
    const char* sbase = smem + (kt & 1) * G1_STAGE;
#pragma unroll
    for (int kk = 0; kk < 2; ++kk) {
      bf16x8 af[4], bfr[6];
#pragma unroll
      for (int i = 0; i < 4; ++i) af[i] = *(const bf16x8*)(sbase + (a_off[i] ^ (kk << 6)));
#pragma unroll
      for (int j = 0; j < 6; ++j) {
        s16x4 lo = tr_read(sbase + b_off[j] + kk * 12288);
        s16x4 hi = tr_read(sbase + (b_off[j] ^ 128) + kk * 12288);
        bfr[j] = __builtin_shufflevector(lo, hi, 0, 1, 2, 3, 4, 5, 6, 7);
      }
#pragma unroll
      for (int i = 0; i < 4; ++i)
#pragma unroll
        for (int j = 0; j < 6; ++j)
          acc[i][j] = __builtin_amdgcn_mfma_f32_16x16x32_bf16(bfr[j], af[i], acc[i][j], 0, 0, 0);
    }
  }

  const int ln4 = (lane >> 4) * 4;
  if (isqkv) {
    const float qscale = 0.22360679774997896f * 1.4426950408889634f;
    char* wtr = smem + wave * 512;
#pragma unroll
    for (int j = 0; j < 6; ++j) {
      const int nb = nblk * 192 + (wn * 6 + j) * 16;
      const int sec = nb / DM;
      if (sec < 2) {
        const int rr = nb + ln4 - sec * DM, head = rr / DH, d = rr - head * DH, d4 = d >> 2;
#pragma unroll
        for (int i = 0; i < 4; ++i) {
          const int tok = m0 + wm * 64 + i * 16 + (lane & 15);
          f32x4 v = acc[i][j];
          if (sec == 0) {
            u32x2 o = {pack_bf2(v[0] * qscale, v[1] * qscale), pack_bf2(v[2] * qscale, v[3] * qscale)};
            *(u32x2*)(Qg + ((size_t)(head * SEQ + tok) * 32 + d)) = o;
          } else {
            char* kb = (char*)Kf + (size_t)(head * 64 + (tok >> 5)) * 1536;
            const int r = tok & 31;
            unsigned p0 = pack_bf2(v[0], v[1]), p1 = pack_bf2(v[2], v[3]);
            if (d4 < 4) {
              u32x2 o = {p0, p1};
              *(u32x2*)(kb + ((d4 >> 1) * 32 + r) * 16 + (d4 & 1) * 8) = o;
            } else {
              u32x4 o = {p0, p1, 0u, 0u};
              *(u32x4*)(kb + 1024 + r * 16) = o;
            }
          }
        }
      } else {
        const int rr = nb + (lane & 15) - 2 * DM, head = rr / DH, d = rr - head * DH;
        const int g = lane >> 4;
#pragma unroll
        for (int i = 0; i < 4; ++i) {
          f32x4 v = acc[i][j];
          u32x2 w = {pack_bf2(v[0], v[1]), pack_bf2(v[2], v[3])};
          *(u32x2*)(wtr + (lane & 15) * 32 + g * 8) = w;
          s16x4 tv = tr_read(wtr + g * 128 + ((lane & 15) >> 2) * 32 + (lane & 3) * 8);
          const int s = (m0 + wm * 64 + i * 16) >> 4;
          u16* vb = Vf + ((size_t)(head * 128 + s) * 48 + (g & 1) * 24 + d) * 8 + (g >> 1) * 4;
          *(s16x4*)vb = tv;
          if (d == DH - 1) { s16x4 one = {0x3F80, 0x3F80, 0x3F80, 0x3F80}; *(s16x4*)(vb + 8) = one; }
        }
      }
    }
  } else {
#pragma unroll
    for (int j = 0; j < 3; ++j) {
      const int f = (nblk - 10) * 96 + (wn * 3 + j) * 16 + ln4;
#pragma unroll
      for (int i = 0; i < 4; ++i) {
        const int tok = m0 + wm * 64 + i * 16 + (lane & 15);
        float hv[4];
#pragma unroll
        for (int r = 0; r < 4; ++r) {
          const float g = acc[i][j][r], u = acc[i][j + 3][r];
          const float e = __builtin_amdgcn_exp2f(g * __builtin_fmaf(g * g, -0.10294324f, -2.30220819f));
          hv[r] = (g * u) * __builtin_amdgcn_rcpf(1.f + e);
        }
        u32x2 o = {pack_bf2(hv[0], hv[1]), pack_bf2(hv[2], hv[3])};
        *(u32x2*)(hbuf + (size_t)tok * DF + f) = o;
      }
    }
  }
}

constexpr int AT_STAGE = 24576;
constexpr float AT_BIAS = 8.f;
#define MFMA32(a, b, c) __builtin_amdgcn_mfma_f32_32x32x16_bf16((a), (b), (c), 0, 0, 0)
__global__ __launch_bounds__(512, 2) void attn_kernel(const u16* __restrict__ Qg, const u16* __restrict__ Kf,
                                                      const u16* __restrict__ Vf, u16* __restrict__ obuf) {
  __shared__ __attribute__((aligned(16))) char smem[3 * AT_STAGE];
  const int b = blockIdx.x;
  const int head = (b & 7) * 4 + ((b >> 3) & 3), qb = b >> 5;
  const int tid = threadIdx.x, wave = tid >> 6, lane = tid & 63;
  const int r = lane & 31, h = lane >> 5;
  const int q = qb * 256 + wave * 32 + r;

  const unsigned lds0 = __builtin_amdgcn_readfirstlane((unsigned)(size_t)smem);
  const unsigned wv = __builtin_amdgcn_readfirstlane(wave);
  const char* gsrc = ((wv < 4) ? (const char*)Kf + wv * 3072 : (const char*)Vf + (wv - 4) * 3072) +
                     (size_t)head * 98304 + lane * 16;
  auto stage = [&](unsigned boff, int tile) {
#pragma unroll
    for (int i = 0; i < 3; ++i) glds16(gsrc + tile * 12288 + i * 1024, lds0 + boff + (wv * 3 + i) * 1024);
  };
  stage(0, 0);
  stage(AT_STAGE, 1);

  const u16* qrow = Qg + (size_t)(head * SEQ + q) * 32;
  bf16x8 q1 = *(const bf16x8*)(qrow + 8 * h);
  bf16x8 q2 = {0, 0, 0, 0, 0, 0, 0, 0};
  {
    s16x4 t4 = *(const s16x4*)(qrow + 16);
    if (h == 0) { q2[0] = t4[0]; q2[1] = t4[1]; q2[2] = t4[2]; q2[3] = t4[3]; }
  }

  const int k1_off = lane * 16, k2_off = 1024 + r * 16;
  const int v_off = 12288 + (h * 24 + (r < 23 ? r : 23)) * 16;

  f32x16 o, negm, s0, s1;
#pragma unroll
  for (int i = 0; i < 16; ++i) { o[i] = 0.f; negm[i] = 0.f; }

  wait_vm0();
  barrier_raw();
  {
    s0 = MFMA32(*(const bf16x8*)(smem + k1_off), q1, negm);
    s0 = MFMA32(*(const bf16x8*)(smem + k2_off), q2, s0);
    s1 = MFMA32(*(const bf16x8*)(smem + 1536 + k1_off), q1, negm);
    s1 = MFMA32(*(const bf16x8*)(smem + 1536 + k2_off), q2, s1);
    float mx = fmaxf(s0[0], s1[0]);
#pragma unroll
    for (int i = 1; i < 16; ++i) mx = fmaxf(mx, fmaxf(s0[i], s1[i]));
    mx = fmaxf(mx, __shfl_xor(mx, 32)) + AT_BIAS;
#pragma unroll
    for (int i = 0; i < 16; ++i) { negm[i] = -mx; s0[i] -= mx; s1[i] -= mx; }
  }
  unsigned cur = 0;
  for (int tile = 0; tile < 8; ++tile) {
    const unsigned nxt = (cur == 2 * AT_STAGE) ? 0 : cur + AT_STAGE;
#pragma unroll
    for (int g = 0; g < 4; ++g) {
      f32x16 n0, n1;
      const char* kb;
      if (g == 3) {
        if (tile < 7) {
          wait_vm0();
          barrier_raw();
          if (tile < 6) stage((nxt == 2 * AT_STAGE) ? 0 : nxt + AT_STAGE, tile + 2);
        }
        kb = smem + nxt;
      } else {
        kb = smem + cur + (g + 1) * 3072;
      }
      n0 = MFMA32(*(const bf16x8*)(kb + k1_off), q1, negm);
      n0 = MFMA32(*(const bf16x8*)(kb + k2_off), q2, n0);
      n1 = MFMA32(*(const bf16x8*)(kb + 1536 + k1_off), q1, negm);
      n1 = MFMA32(*(const bf16x8*)(kb + 1536 + k2_off), q2, n1);
      unsigned p[16];
#pragma unroll
      for (int i = 0; i < 8; ++i) {
        p[i] = pack_bf2(__builtin_amdgcn_exp2f(s0[2 * i]), __builtin_amdgcn_exp2f(s0[2 * i + 1]));
        p[8 + i] = pack_bf2(__builtin_amdgcn_exp2f(s1[2 * i]), __builtin_amdgcn_exp2f(s1[2 * i + 1]));
      }
      unsigned orr = p[0];
#pragma unroll
      for (int i = 1; i < 16; ++i) orr |= p[i];
      if (__builtin_amdgcn_ballot_w64((orr & 0x40004000u) != 0) != 0) {
        float mx = fmaxf(s0[0], s1[0]);
#pragma unroll
        for (int i = 1; i < 16; ++i) mx = fmaxf(mx, fmaxf(s0[i], s1[i]));
        mx = fmaxf(mx, __shfl_xor(mx, 32));
        const float delta = fmaxf(mx + AT_BIAS, 0.f);
        const float alpha = __builtin_amdgcn_exp2f(-delta);
#pragma unroll
        for (int i = 0; i < 16; ++i) {
          negm[i] -= delta; s0[i] -= delta; s1[i] -= delta; n0[i] -= delta; n1[i] -= delta; o[i] *= alpha;
        }
#pragma unroll
        for (int i = 0; i < 8; ++i) {
          p[i] = pack_bf2(__builtin_amdgcn_exp2f(s0[2 * i]), __builtin_amdgcn_exp2f(s0[2 * i + 1]));
          p[8 + i] = pack_bf2(__builtin_amdgcn_exp2f(s1[2 * i]), __builtin_amdgcn_exp2f(s1[2 * i + 1]));
        }
      }
#pragma unroll
      for (int s = 0; s < 4; ++s) {
        u32x4 pv = {p[4 * s], p[4 * s + 1], p[4 * s + 2], p[4 * s + 3]};
        bf16x8 vf = *(const bf16x8*)(smem + cur + (g * 4 + s) * 768 + v_off);
        o = MFMA32(vf, __builtin_bit_cast(bf16x8, pv), o);
      }
      s0 = n0; s1 = n1;
    }
    cur = nxt;
  }
  const float l = __shfl(o[8], r + 32);
  const float inv = 1.f / l;
  u16* orow = obuf + (size_t)q * DM + head * DH + 4 * h;
  {
    u32x2 w0 = {pack_bf2(o[0] * inv, o[1] * inv), pack_bf2(o[2] * inv, o[3] * inv)};
    u32x2 w1 = {pack_bf2(o[4] * inv, o[5] * inv), pack_bf2(o[6] * inv, o[7] * inv)};
    *(u32x2*)(orow) = w0;
    *(u32x2*)(orow + 8) = w1;
    if (h == 0) {
      u32x2 w2 = {pack_bf2(o[8] * inv, o[9] * inv), pack_bf2(o[10] * inv, o[11] * inv)};
      *(u32x2*)(orow + 16) = w2;
    }
  }
}

constexpr int G2_STAGE = 36864;
__global__ __launch_bounds__(256, 1) void gemm2_kernel(const u16* __restrict__ obuf, const u16* __restrict__ hbuf,
                                                       const u16* __restrict__ wo, const u16* __restrict__ wd,
                                                       const u16* __restrict__ x, u16* __restrict__ out) {
  __shared__ __attribute__((aligned(16))) char smem[4 * G2_STAGE];
  const int b = blockIdx.x;
  const int t = (b & 7) * 32 + (b >> 3);
  const int nblk = t & 7, mblk = t >> 3;
  const int tid = threadIdx.x, wave = tid >> 6, lane = tid & 63;
  const int m0 = mblk * 64, n0 = nblk * 80;

  int a_row[4], a_c[4];
#pragma unroll
  for (int i = 0; i < 4; ++i) {
    int row = (wave * 4 + i) * 4 + (lane >> 4);
    a_row[i] = m0 + row;
    a_c[i] = ((lane & 15) ^ (row & 15)) * 8;
  }
  int b_o[5];
#pragma unroll
  for (int i = 0; i < 5; ++i) {
    int sid = (wave * 5 + i) * 4 + (lane >> 4);
    int kb = sid / 5, nt = sid - kb * 5;
    int rr = ((lane >> 1) & 7) ^ ((kb & 1) << 2);
    b_o[i] = (kb * 8 + rr) * DM + n0 + nt * 16 + (lane & 1) * 8;
  }
  const unsigned lds0 = __builtin_amdgcn_readfirstlane((unsigned)(size_t)smem);
  const unsigned wv = __builtin_amdgcn_readfirstlane(wave);
  auto stage = [&](int buf, int st) {
    unsigned sa = lds0 + buf * G2_STAGE;
    unsigned sb = sa + 16384;
    const u16* A; const u16* B; int lda, k0;
    if (st < 5) { A = obuf; lda = DM; k0 = st * 128; B = wo; }
    else { A = hbuf; lda = DF; k0 = (st - 5) * 128; if (k0 > DF - 128) k0 = DF - 128; B = wd; }
#pragma unroll
    for (int i = 0; i < 4; ++i) glds16(A + (size_t)a_row[i] * lda + k0 + a_c[i], sa + (wv * 4 + i) * 1024);
#pragma unroll
    for (int i = 0; i < 5; ++i) glds16(B + (size_t)k0 * DM + b_o[i], sb + (wv * 5 + i) * 1024);
  };

  int a_off[4];
#pragma unroll
  for (int i = 0; i < 4; ++i) {
    int row = i * 16 + (lane & 15);
    a_off[i] = row * 256 + (((wave * 4 + (lane >> 4)) ^ (row & 15)) << 4);
  }
  int b_off;
  {
    int g = lane >> 4, q = (lane & 15) >> 2, p = lane & 3;
    b_off = 16384 + (wave * 4 + g) * 1280 + ((g & 1) << 7) + q * 32 + p * 8;
  }
  f32x4 acc[4][5];
#pragma unroll
  for (int i = 0; i < 4; ++i)
#pragma unroll
    for (int j = 0; j < 5; ++j) acc[i][j] = (f32x4){0.f, 0.f, 0.f, 0.f};

  constexpr int NST = 5 + 14;
  stage(0, 0);
  stage(1, 1);
  stage(2, 2);
  for (int st = 0; st < NST; ++st) {
    if (st + 2 < NST) asm volatile("s_waitcnt vmcnt(18)" ::: "memory");
    else if (st + 1 < NST) asm volatile("s_waitcnt vmcnt(9)" ::: "memory");
    else wait_vm0();
    barrier_raw();
    if (st + 3 < NST) stage((st + 3) & 3, st + 3);
    const char* sbase = smem + (st & 3) * G2_STAGE;
    if (st == NST - 1 && wave < 2) continue;
    bf16x8 af[4], bfr[5];
#pragma unroll
    for (int i = 0; i < 4; ++i) af[i] = *(const bf16x8*)(sbase + a_off[i]);
#pragma unroll
    for (int j = 0; j < 5; ++j) {
      s16x4 lo = tr_read(sbase + b_off + j * 256);
      s16x4 hi = tr_read(sbase + (b_off ^ 128) + j * 256);
      bfr[j] = __builtin_shufflevector(lo, hi, 0, 1, 2, 3, 4, 5, 6, 7);
    }
#pragma unroll
    for (int i = 0; i < 4; ++i)
#pragma unroll
      for (int j = 0; j < 5; ++j)
        acc[i][j] = __builtin_amdgcn_mfma_f32_16x16x32_bf16(bfr[j], af[i], acc[i][j], 0, 0, 0);
  }
  __syncthreads();
  float* red = (float*)smem;
#pragma unroll
  for (int i = 0; i < 4; ++i)
#pragma unroll
    for (int j = 0; j < 5; ++j)
      *(f32x4*)(red + (wave * 64 + i * 16 + (lane & 15)) * 80 + j * 16 + (lane >> 4) * 4) = acc[i][j];
  __syncthreads();
#pragma unroll
  for (int i = 0; i < 5; ++i) {
    int item = tid + 256 * i;
    int row = item / 20, c4 = item - row * 20;
    f32x4 s = *(const f32x4*)(red + row * 80 + c4 * 4);
#pragma unroll
    for (int w = 1; w < 4; ++w) s += *(const f32x4*)(red + (w * 64 + row) * 80 + c4 * 4);
    size_t gi = (size_t)(m0 + row) * DM + n0 + c4 * 4;
    u32x2 xv = *(const u32x2*)(x + gi);
    u32x2 o = {pack_bf2(s[0] + bf_lo(xv[0]), s[1] + bf_hi(xv[0])), pack_bf2(s[2] + bf_lo(xv[1]), s[3] + bf_hi(xv[1]))};
    *(u32x2*)(out + gi) = o;
  }
}

extern "C" void kernel_launch(void* const* d_in, const int* in_sizes, int n_in, void* d_out, int out_size,
                              void* d_ws, size_t ws_size, hipStream_t stream) {
  const u16* x = (const u16*)d_in[0];
  const u16* wqkv = (const u16*)d_in[1];
  const u16* wo = (const u16*)d_in[2];
  const u16* wg = (const u16*)d_in[4];
  const u16* wu = (const u16*)d_in[5];
  const u16* wd = (const u16*)d_in[6];
  char* ws = (char*)d_ws;
  u16* xn = (u16*)(ws);
  u16* Qg = (u16*)(ws + (4u << 20));
  u16* Kf = (u16*)(ws + (8u << 20));
  u16* Vf = (u16*)(ws + (12u << 20));
  u16* hb = (u16*)(ws + (16u << 20));
  u16* ob = (u16*)(ws + (24u << 20));
  ln_kernel<<<SEQ / 4, 256, 0, stream>>>(x, xn);
  gemm1_kernel<<<224, 512, 0, stream>>>(xn, wqkv, wg, wu, Qg, Kf, Vf, hb);
  attn_kernel<<<256, 512, 0, stream>>>(Qg, Kf, Vf, ob);
  gemm2_kernel<<<256, 256, 0, stream>>>(ob, hb, wo, wd, x, (u16*)d_out);
}
```

```cpp
#include <hip/hip_runtime.h>
#include <stdint.h>

typedef __attribute__((ext_vector_type(8))) short bf16x8;
typedef __attribute__((ext_vector_type(4))) short s16x4;
typedef __attribute__((ext_vector_type(4))) float f32x4;
typedef __attribute__((ext_vector_type(2))) float f32x2;
typedef __attribute__((ext_vector_type(16))) float f32x16;
typedef __attribute__((ext_vector_type(2))) __bf16 bf2_t;
typedef __attribute__((ext_vector_type(4))) unsigned u32x4;
typedef __attribute__((ext_vector_type(2))) unsigned u32x2;
typedef unsigned short u16;
typedef __attribute__((address_space(3))) s16x4 lds_s16x4;

#define DI __device__ __forceinline__

constexpr int SEQ = 2048, DM = 640, NH = 32, DH = 20, DF = 1728;

DI unsigned pack_bf2(float a, float b) {
  f32x2 v = {a, b};
  return __builtin_bit_cast(unsigned, __builtin_convertvector(v, bf2_t));
}
DI float bf_lo(unsigned u) { return __uint_as_float(u << 16); }
DI float bf_hi(unsigned u) { return __uint_as_float(u & 0xffff0000u); }
DI float rbf(float x) { return bf_lo(pack_bf2(x, 0.f)); }

DI void glds16(const void* g, unsigned lds_addr) {
  unsigned keep;
  asm volatile("s_mov_b32 %0, m0\n\ts_mov_b32 m0, %2\n\ts_nop 0\n\tglobal_load_lds_dwordx4 %1, off\n\ts_mov_b32 m0, %0"
               : "=&s"(keep) : "v"(g), "s"(lds_addr) : "memory");
}
DI void wait_vm0() { asm volatile("s_waitcnt vmcnt(0)" ::: "memory"); }
DI void barrier_raw() {
  asm volatile("s_waitcnt lgkmcnt(0)" ::: "memory");
  __builtin_amdgcn_s_barrier();
  asm volatile("" ::: "memory");
}
DI int imax3(int a, int b, int c) { return max(a, max(b, c)); }
DI s16x4 tr_read(const char* lds) {
  return __builtin_amdgcn_ds_read_tr16_b64_v4i16((lds_s16x4*)lds);
}

__global__ __launch_bounds__(256) void ln_kernel(const u16* __restrict__ x, u16* __restrict__ xn) {
  const int row = blockIdx.x * 4 + (threadIdx.x >> 6), lane = threadIdx.x & 63;
  const unsigned* xr = (const unsigned*)(x + row * DM);
  float f[10];
#pragma unroll
  for (int j = 0; j < 5; ++j) {
    unsigned v = xr[lane + 64 * j];
    f[2 * j] = bf_lo(v);
    f[2 * j + 1] = bf_hi(v);
  }
  float s = 0.f;
#pragma unroll
  for (int j = 0; j < 10; ++j) s += f[j];
#pragma unroll
  for (int o = 32; o > 0; o >>= 1) s += __shfl_xor(s, o);
  const float mean = s * (1.f / DM);
  float q = 0.f;
#pragma unroll
  for (int j = 0; j < 10; ++j) { float d = f[j] - mean; q += d * d; }
#pragma unroll
  for (int o = 32; o > 0; o >>= 1) q += __shfl_xor(q, o);
  const float mb = rbf(mean);
  const float vb = rbf(q * (1.f / DM));
  const float den = rbf(sqrtf(rbf(vb + 1e-5f)));
  unsigned* o = (unsigned*)(xn + row * DM);
#pragma unroll
  for (int j = 0; j < 5; ++j)
    o[lane + 64 * j] = pack_bf2(rbf(f[2 * j] - mb) / den, rbf(f[2 * j + 1] - mb) / den);
}

constexpr int G1_BOFF = 3 * 32768;
__global__ __launch_bounds__(512, 2) void gemm1_kernel(const u16* __restrict__ xn, const u16* __restrict__ wqkv,
                                                       const u16* __restrict__ wg, const u16* __restrict__ wu,
                                                       u16* __restrict__ Qg, u16* __restrict__ Kf,
                                                       u16* __restrict__ Vf, u16* __restrict__ hbuf) {
  __shared__ __attribute__((aligned(16))) char smem[G1_BOFF + 2 * 24576];
  const int b = blockIdx.x;
  const int t = (b & 7) * 28 + (b >> 3);
  const int nblk = t >> 3, mblk = t & 7;
  const int tid = threadIdx.x, wave = tid >> 6, lane = tid & 63;
  const int wm = wave >> 1, wn = wave & 1;
  const int m0 = mblk * 256;
  const bool isqkv = nblk < 10;

  const u16* a_src[4];
#pragma unroll
  for (int i = 0; i < 4; ++i) {
    int row = wave * 32 + i * 8 + (lane >> 3);
    int c = (lane & 7) ^ ((row >> 1) & 7);
    a_src[i] = xn + (size_t)(m0 + row) * DM + c * 8;
  }
  const int ldb = isqkv ? 3 * DM : DF;
  const u16* b_src[3];
#pragma unroll
  for (int i = 0; i < 3; ++i) {
    int kb = wave;
    int nt = i * 4 + (lane >> 4);
    int rr = ((lane >> 1) & 7) ^ ((kb & 1) << 2);
    int k = kb * 8 + rr;
    const u16* base;
    if (isqkv) base = wqkv + nblk * 192 + nt * 16;
    else base = (nt < 6) ? (wg + (nblk - 10) * 96 + nt * 16) : (wu + (nblk - 10) * 96 + (nt - 6) * 16);
    b_src[i] = base + (size_t)k * ldb + (lane & 1) * 8;
  }
  const unsigned lds0 = __builtin_amdgcn_readfirstlane((unsigned)(size_t)smem);
  const unsigned wv = __builtin_amdgcn_readfirstlane(wave);
  auto stage_a = [&](int kt, int g) { glds16(a_src[g] + kt * 64, lds0 + (kt % 3) * 32768 + (wv * 4 + g) * 1024); };
  auto stage_b = [&](int kt, int g) {
    glds16(b_src[g] + (size_t)kt * 64 * ldb, lds0 + G1_BOFF + (kt & 1) * 24576 + (wv * 3 + g) * 1024);
  };
#pragma unroll
  for (int kt = 0; kt < 2; ++kt) {
#pragma unroll
    for (int g = 0; g < 4; ++g) stage_a(kt, g);
#pragma unroll
    for (int g = 0; g < 3; ++g) stage_b(kt, g);
  }

  const int a_off = (wm * 64 + (lane & 15)) * 128 + (((lane >> 4) ^ ((lane >> 1) & 7)) << 4);
  int b_off[2];
  {
    int g = lane >> 4, q = (lane & 15) >> 2, p = lane & 3;
    int base = G1_BOFF + g * 3072 + ((g & 1) << 7) + q * 32 + p * 8;
    b_off[0] = base + (isqkv ? wn * 6 : wn * 3) * 256;
    b_off[1] = base + (isqkv ? wn * 6 + 3 : 6 + wn * 3) * 256;
  }

  f32x4 acc[4][6];
#pragma unroll
  for (int i = 0; i < 4; ++i)
#pragma unroll
    for (int j = 0; j < 6; ++j) acc[i][j] = (f32x4){0.f, 0.f, 0.f, 0.f};

  bf16x8 af0[4], bf0[6], af1[4], bf1[6];
  auto read_q = [&](bf16x8 (&af)[4], bf16x8 (&bfr)[6], int kt, int kk, int g) {
    const char* sa = smem + (kt % 3) * 32768;
    const char* sb = smem + (kt & 1) * 24576;
    auto rb = [&](int j) {
      s16x4 lo = tr_read(sb + b_off[j / 3] + (j % 3) * 256 + kk * 12288);
      s16x4 hi = tr_read(sb + (b_off[j / 3] ^ 128) + (j % 3) * 256 + kk * 12288);
      bfr[j] = __builtin_shufflevector(lo, hi, 0, 1, 2, 3, 4, 5, 6, 7);
    };
    auto ra = [&](int i) { af[i] = *(const bf16x8*)(sa + (a_off ^ (kk << 6)) + i * 2048); };
    if (g == 0) { ra(0); rb(0); rb(1); }
    if (g == 1) { rb(2); rb(3); }
    if (g == 2) { rb(4); rb(5); }
    if (g == 3) { ra(1); ra(2); ra(3); }
  };
  auto mma_q = [&](bf16x8 (&af)[4], bf16x8 (&bfr)[6], int i) {
#pragma unroll
    for (int j = 0; j < 6; ++j)
      acc[i][j] = __builtin_amdgcn_mfma_f32_16x16x32_bf16(bfr[j], af[i], acc[i][j], 0, 0, 0);
  };

  asm volatile("s_waitcnt vmcnt(7)" ::: "memory");
  barrier_raw();
#pragma unroll
  for (int g = 0; g < 4; ++g) read_q(af0, bf0, 0, 0, g);
#pragma unroll
  for (int kt = 0; kt < 10; ++kt) {
#pragma unroll
    for (int g = 0; g < 4; ++g) {
      __builtin_amdgcn_sched_barrier(0);
      read_q(af1, bf1, kt, 1, g);
      mma_q(af0, bf0, g);
      __builtin_amdgcn_sched_barrier(0);
      if (kt + 2 < 10) stage_a(kt + 2, g);
    }
    __builtin_amdgcn_sched_barrier(0);
    if (kt + 1 < 10) {
      if (kt + 2 < 10) asm volatile("s_waitcnt vmcnt(4)" ::: "memory");
      else wait_vm0();
      barrier_raw();
    }
#pragma unroll
    for (int g = 0; g < 4; ++g) {
      __builtin_amdgcn_sched_barrier(0);
      if (kt + 1 < 10) read_q(af0, bf0, kt + 1, 0, g);
      mma_q(af1, bf1, g);
      __builtin_amdgcn_sched_barrier(0);
      if (kt + 2 < 10 && g < 3) stage_b(kt + 2, g);
    }
  }
  __builtin_amdgcn_sched_barrier(0);
  barrier_raw();

  int tz = threadIdx.x;
  asm volatile("" : "+v"(tz));
  const int e_wave = tz >> 6, e_lane = tz & 63, e_wm = e_wave >> 1, e_wn = e_wave & 1;
  const int ln4 = (e_lane >> 4) * 4;
  if (isqkv) {
    const float qscale = 0.22360679774997896f * 1.4426950408889634f;
    char* wtr = smem + e_wave * 512;
#pragma unroll
    for (int j = 0; j < 6; ++j) {
      const int nb = nblk * 192 + (e_wn * 6 + j) * 16;
      const int sec = nb / DM;
      if (sec < 2) {
        const int rr = nb + ln4 - sec * DM, head = rr / DH, d = rr - head * DH, d4 = d >> 2;
#pragma unroll
        for (int i = 0; i < 4; ++i) {
          const int tok = m0 + e_wm * 64 + i * 16 + (e_lane & 15);
          f32x4 v = acc[i][j];
          if (sec == 0) {
            u32x2 o = {pack_bf2(v[0] * qscale, v[1] * qscale), pack_bf2(v[2] * qscale, v[3] * qscale)};
            *(u32x2*)(Qg + ((size_t)(head * SEQ + tok) * 32 + d)) = o;
          } else {
            char* kb = (char*)Kf + (size_t)(head * 64 + (tok >> 5)) * 1536;
            const int r = tok & 31;
            unsigned p0 = pack_bf2(v[0], v[1]), p1 = pack_bf2(v[2], v[3]);
            if (d4 < 4) {
              u32x2 o = {p0, p1};
              *(u32x2*)(kb + ((d4 >> 1) * 32 + r) * 16 + (d4 & 1) * 8) = o;
            } else {
              u32x4 o = {p0, p1, 0x00003F80u, 0u};
              *(u32x4*)(kb + 1024 + r * 16) = o;
            }
          }
        }
      } else {
        const int rr = nb + (e_lane & 15) - 2 * DM, head = rr / DH, d = rr - head * DH;
        const int g = e_lane >> 4;
#pragma unroll
        for (int i = 0; i < 4; ++i) {
          f32x4 v = acc[i][j];
          u32x2 w = {pack_bf2(v[0], v[1]), pack_bf2(v[2], v[3])};
          *(u32x2*)(wtr + (e_lane & 15) * 32 + g * 8) = w;
          s16x4 tv = tr_read(wtr + g * 128 + ((e_lane & 15) >> 2) * 32 + (e_lane & 3) * 8);
          const int s = (m0 + e_wm * 64 + i * 16) >> 4;
          u16* vb = Vf + ((size_t)(head * 128 + s) * 48 + (g & 1) * 24 + d) * 8 + (g >> 1) * 4;
          *(s16x4*)vb = tv;
          if (d == DH - 1) { s16x4 one = {0x3F80, 0x3F80, 0x3F80, 0x3F80}; *(s16x4*)(vb + 8) = one; }
        }
      }
    }
  } else {
#pragma unroll
    for (int j = 0; j < 3; ++j) {
      const int f = (nblk - 10) * 96 + (e_wn * 3 + j) * 16 + ln4;
#pragma unroll
      for (int i = 0; i < 4; ++i) {
        const int tok = m0 + e_wm * 64 + i * 16 + (e_lane & 15);
        float hv[4];
#pragma unroll
        for (int r = 0; r < 4; ++r) {
          const float g = acc[i][j][r], u = acc[i][j + 3][r];
          const float e = __builtin_amdgcn_exp2f(g * __builtin_fmaf(g * g, -0.10294324f, -2.30220819f));
          hv[r] = (g * u) * __builtin_amdgcn_rcpf(1.f + e);
        }
        u32x2 o = {pack_bf2(hv[0], hv[1]), pack_bf2(hv[2], hv[3])};
        *(u32x2*)(hbuf + (size_t)tok * DF + f) = o;
      }
    }
  }
}

constexpr int AT_STAGE = 24576;
constexpr float AT_BIAS = 8.f;
#define MFMA32(a, b, c) __builtin_amdgcn_mfma_f32_32x32x16_bf16((a), (b), (c), 0, 0, 0)
#define MFMA16(a, b, c) __builtin_amdgcn_mfma_f32_16x16x32_bf16((a), (b), (c), 0, 0, 0)

DI void attn_role(char* smem, int b, const u16* __restrict__ Qg, const u16* __restrict__ Kf,
                  const u16* __restrict__ Vf, u16* __restrict__ obuf) {
  const int head = (b & 7) * 4 + ((b >> 3) & 3), qb = b >> 5;
  const int tid = threadIdx.x, wave = tid >> 6, lane = tid & 63;
  const int r = lane & 31, h = lane >> 5;
  const int q = qb * 256 + wave * 32 + r;

  const unsigned lds0 = __builtin_amdgcn_readfirstlane((unsigned)(size_t)smem);
  const unsigned wv = __builtin_amdgcn_readfirstlane(wave);
  const char* gsrc = ((wv < 4) ? (const char*)Kf + wv * 3072 : (const char*)Vf + (wv - 4) * 3072) +
                     (size_t)head * 98304 + lane * 16;
  auto stage = [&](unsigned boff, int tile) {
#pragma unroll
    for (int i = 0; i < 3; ++i) glds16(gsrc + tile * 12288 + i * 1024, lds0 + boff + (wv * 3 + i) * 1024);
  };
  const u16* qrow = Qg + (size_t)(head * SEQ + q) * 32;
  bf16x8 q1 = *(const bf16x8*)(qrow + 8 * h);
  u32x4 q2 = {0u, 0u, 0u, 0u};
  {
    u32x2 t2 = *(const u32x2*)(qrow + 16);
    if (h == 0) { q2[0] = t2[0]; q2[1] = t2[1]; }
  }
  stage(0, 0);
  stage(AT_STAGE, 1);

  const int k1_off = lane * 16, k2_off = 1024 + r * 16;
  const int v_off = 12288 + (h * 24 + (r < 23 ? r : 23)) * 16;

  f32x16 o, s0, s1;
  const f32x16 zero = {0.f, 0.f, 0.f, 0.f, 0.f, 0.f, 0.f, 0.f, 0.f, 0.f, 0.f, 0.f, 0.f, 0.f, 0.f, 0.f};
  o = zero;
  float mref;

  asm volatile("s_waitcnt vmcnt(3)" ::: "memory");
  barrier_raw();
  {
    s0 = MFMA32(*(const bf16x8*)(smem + k1_off), q1, zero);
    s0 = MFMA32(*(const bf16x8*)(smem + k2_off), __builtin_bit_cast(bf16x8, q2), s0);
    s1 = MFMA32(*(const bf16x8*)(smem + 1536 + k1_off), q1, zero);
    s1 = MFMA32(*(const bf16x8*)(smem + 1536 + k2_off), __builtin_bit_cast(bf16x8, q2), s1);
    float mx = fmaxf(s0[0], s1[0]);
#pragma unroll
    for (int i = 1; i < 16; ++i) mx = fmaxf(mx, fmaxf(s0[i], s1[i]));
    mref = rbf(fmaxf(mx, __shfl_xor(mx, 32)) + AT_BIAS);
    if (h == 0) q2[2] = pack_bf2(-mref, 0.f);
#pragma unroll
    for (int i = 0; i < 16; ++i) { s0[i] -= mref; s1[i] -= mref; }
  }
  unsigned cur = 0;
  for (int tile = 0; tile < 8; ++tile) {
    const unsigned nxt = (cur == 2 * AT_STAGE) ? 0 : cur + AT_STAGE;
#pragma unroll
    for (int g = 0; g < 4; ++g) {
      f32x16 n0, n1;
      const char* kb;
      if (g == 3) {
        if (tile < 7) {
          wait_vm0();
          barrier_raw();
          if (tile < 6) stage((nxt == 2 * AT_STAGE) ? 0 : nxt + AT_STAGE, tile + 2);
        }
        kb = smem + nxt;
      } else {
        kb = smem + cur + (g + 1) * 3072;
      }
      n0 = MFMA32(*(const bf16x8*)(kb + k1_off), q1, zero);
      n0 = MFMA32(*(const bf16x8*)(kb + k2_off), __builtin_bit_cast(bf16x8, q2), n0);
      n1 = MFMA32(*(const bf16x8*)(kb + 1536 + k1_off), q1, zero);
      n1 = MFMA32(*(const bf16x8*)(kb + 1536 + k2_off), __builtin_bit_cast(bf16x8, q2), n1);
      unsigned p[16];
#pragma unroll
      for (int i = 0; i < 8; ++i) {
        p[i] = pack_bf2(__builtin_amdgcn_exp2f(s0[2 * i]), __builtin_amdgcn_exp2f(s0[2 * i + 1]));
        p[8 + i] = pack_bf2(__builtin_amdgcn_exp2f(s1[2 * i]), __builtin_amdgcn_exp2f(s1[2 * i + 1]));
      }
      unsigned orr = p[0];
#pragma unroll
      for (int i = 1; i < 16; ++i) orr |= p[i];
      if (__builtin_amdgcn_ballot_w64((orr & 0x40004000u) != 0) != 0) {
        float mx = fmaxf(s0[0], s1[0]);
#pragma unroll
        for (int i = 1; i < 16; ++i) mx = fmaxf(mx, fmaxf(s0[i], s1[i]));
        mx = fmaxf(mx, __shfl_xor(mx, 32));
        const float mnew = rbf(mref + fmaxf(mx + AT_BIAS, 0.f));
        const float delta = mnew - mref;
        const float alpha = __builtin_amdgcn_exp2f(-delta);
        mref = mnew;
        if (h == 0) q2[2] = pack_bf2(-mref, 0.f);
#pragma unroll
        for (int i = 0; i < 16; ++i) {
          s0[i] -= delta; s1[i] -= delta; n0[i] -= delta; n1[i] -= delta; o[i] *= alpha;
        }
#pragma unroll
        for (int i = 0; i < 8; ++i) {
          p[i] = pack_bf2(__builtin_amdgcn_exp2f(s0[2 * i]), __builtin_amdgcn_exp2f(s0[2 * i + 1]));
          p[8 + i] = pack_bf2(__builtin_amdgcn_exp2f(s1[2 * i]), __builtin_amdgcn_exp2f(s1[2 * i + 1]));
        }
      }
#pragma unroll
      for (int s = 0; s < 4; ++s) {
        u32x4 pv = {p[4 * s], p[4 * s + 1], p[4 * s + 2], p[4 * s + 3]};
        bf16x8 vf = *(const bf16x8*)(smem + cur + (g * 4 + s) * 768 + v_off);
        o = MFMA32(vf, __builtin_bit_cast(bf16x8, pv), o);
      }
      s0 = n0; s1 = n1;
    }
    cur = nxt;
  }
  const float l = __shfl(o[8], r + 32);
  const float inv = 1.f / l;
  u16* orow = obuf + (size_t)q * DM + head * DH + 4 * h;
  {
    u32x2 w0 = {pack_bf2(o[0] * inv, o[1] * inv), pack_bf2(o[2] * inv, o[3] * inv)};
    u32x2 w1 = {pack_bf2(o[4] * inv, o[5] * inv), pack_bf2(o[6] * inv, o[7] * inv)};
    *(u32x2*)(orow) = w0;
    *(u32x2*)(orow + 8) = w1;
    if (h == 0) {
      u32x2 w2 = {pack_bf2(o[8] * inv, o[9] * inv), pack_bf2(o[10] * inv, o[11] * inv)};
      *(u32x2*)(orow + 16) = w2;
    }
  }
}

constexpr int GS_STAGE = 18432;
template <int NST, int LDA, bool FINAL>
DI void gemm_small_role(char* smem, int b, const u16* __restrict__ A, const u16* __restrict__ B,
                        const u16* __restrict__ x, float* __restrict__ y, u16* __restrict__ out) {
  const int t = (b & 7) * 32 + (b >> 3);
  const int nblk = t & 7, mblk = t >> 3;
  const int tid = threadIdx.x, wave = tid >> 6, lane = tid & 63;
  const int m0 = mblk * 64, n0 = nblk * 80;
  const unsigned lds0 = __builtin_amdgcn_readfirstlane((unsigned)(size_t)smem);
  const unsigned wv = __builtin_amdgcn_readfirstlane(wave);

  const u16* a_src;
  {
    int row = wave * 8 + (lane >> 3);
    a_src = A + (size_t)(m0 + row) * LDA + ((lane & 7) ^ ((row >> 1) & 7)) * 8;
  }
  const u16* b_src = B + (wave * 8 + (((lane >> 1) & 7) ^ ((wave & 1) << 2))) * DM + n0 + (lane >> 4) * 16 + (lane & 1) * 8;
  auto stage = [&](int st) {
    unsigned sa = lds0 + (st & 3) * GS_STAGE;
    glds16(a_src + st * 64, sa + wv * 1024);
    glds16(b_src + (size_t)st * 64 * DM, sa + 8192 + wv * 1280);
    if (lane < 16) glds16(b_src + (size_t)st * 64 * DM + 64, sa + 8192 + wv * 1280 + 1024);
  };
  stage(0);
  stage(1);
  stage(2);
  const int kk = wave & 1, wm = wave >> 1;
  const int a_off = (wm * 16 + (lane & 15)) * 128 + (((kk * 4 + (lane >> 4)) ^ ((lane >> 1) & 7)) << 4);
  int b_off;
  {
    int g = lane >> 4, q = (lane & 15) >> 2, p = lane & 3;
    b_off = 8192 + (kk * 4 + g) * 1280 + ((g & 1) << 7) + q * 32 + p * 8;
  }
  f32x4 acc[5];
#pragma unroll
  for (int j = 0; j < 5; ++j) acc[j] = (f32x4){0.f, 0.f, 0.f, 0.f};

  for (int st = 0; st < NST; ++st) {
    if (st + 2 < NST) asm volatile("s_waitcnt vmcnt(6)" ::: "memory");
    else if (st + 1 < NST) asm volatile("s_waitcnt vmcnt(3)" ::: "memory");
    else wait_vm0();
    barrier_raw();
    if (st + 3 < NST) stage(st + 3);
    const char* sbase = smem + (st & 3) * GS_STAGE;
    bf16x8 af = *(const bf16x8*)(sbase + a_off);
    bf16x8 bfr[5];
#pragma unroll
    for (int j = 0; j < 5; ++j) {
      s16x4 lo = tr_read(sbase + b_off + j * 256);
      s16x4 hi = tr_read(sbase + (b_off ^ 128) + j * 256);
      bfr[j] = __builtin_shufflevector(lo, hi, 0, 1, 2, 3, 4, 5, 6, 7);
    }
#pragma unroll
    for (int j = 0; j < 5; ++j) acc[j] = MFMA16(bfr[j], af, acc[j]);
  }
  barrier_raw();
  float* red = (float*)smem + (wm * 64 + lane) * 20;
  if (kk == 1) {
#pragma unroll
    for (int j = 0; j < 5; ++j) *(f32x4*)(red + j * 4) = acc[j];
  }
  barrier_raw();
  if (kk == 0) {
    const size_t gi = (size_t)(m0 + wm * 16 + (lane & 15)) * DM + n0 + (lane >> 4) * 4;
#pragma unroll
    for (int j = 0; j < 5; ++j) {
      f32x4 s = acc[j] + *(const f32x4*)(red + j * 4);
      if (FINAL) {
        s += *(const f32x4*)(y + gi + j * 16);
        u32x2 o = {pack_bf2(s[0], s[1]), pack_bf2(s[2], s[3])};
        *(u32x2*)(out + gi + j * 16) = o;
      } else {
        u32x2 xv = *(const u32x2*)(x + gi + j * 16);
        s[0] += bf_lo(xv[0]); s[1] += bf_hi(xv[0]); s[2] += bf_lo(xv[1]); s[3] += bf_hi(xv[1]);
        *(f32x4*)(y + gi + j * 16) = s;
      }
    }
  }
}

__global__ __launch_bounds__(512, 4) void k2_kernel(const u16* __restrict__ Qg, const u16* __restrict__ Kf,
                                                    const u16* __restrict__ Vf, u16* __restrict__ obuf,
                                                    const u16* __restrict__ hbuf, const u16* __restrict__ wd,
                                                    const u16* __restrict__ x, float* __restrict__ y) {
  __shared__ __attribute__((aligned(16))) char smem[3 * AT_STAGE];
  const int b = blockIdx.x;
  if (b < 256) attn_role(smem, b, Qg, Kf, Vf, obuf);
  else gemm_small_role<DF / 64, DF, false>(smem, b - 256, hbuf, wd, x, y, nullptr);
}

__global__ __launch_bounds__(512, 2) void k3_kernel(const u16* __restrict__ obuf, const u16* __restrict__ wo,
                                                    float* __restrict__ y, u16* __restrict__ out) {
  __shared__ __attribute__((aligned(16))) char smem[4 * GS_STAGE];
  gemm_small_role<DM / 64, DM, true>(smem, blockIdx.x, obuf, wo, nullptr, y, out);
}

extern "C" void kernel_launch(void* const* d_in, const int* in_sizes, int n_in, void* d_out, int out_size,
                              void* d_ws, size_t ws_size, hipStream_t stream) {
  const u16* x = (const u16*)d_in[0];
  const u16* wqkv = (const u16*)d_in[1];
  const u16* wo = (const u16*)d_in[2];
  const u16* wg = (const u16*)d_in[4];
  const u16* wu = (const u16*)d_in[5];
  const u16* wd = (const u16*)d_in[6];
  char* ws = (char*)d_ws;
  u16* xn = (u16*)(ws);
  u16* Qg = (u16*)(ws + (4u << 20));
  u16* Kf = (u16*)(ws + (8u << 20));
  u16* Vf = (u16*)(ws + (12u << 20));
  u16* hb = (u16*)(ws + (16u << 20));
  u16* ob = (u16*)(ws + (24u << 20));
  float* yb = (float*)(ws + (28u << 20));
  ln_kernel<<<SEQ / 4, 256, 0, stream>>>(x, xn);
  gemm1_kernel<<<224, 512, 0, stream>>>(xn, wqkv, wg, wu, Qg, Kf, Vf, hb);
  k2_kernel<<<512, 512, 0, stream>>>(Qg, Kf, Vf, ob, hb, wd, x, yb);
  k3_kernel<<<256, 512, 0, stream>>>(ob, wo, yb, (u16*)d_out);
}
```

```cpp
#include <hip/hip_runtime.h>
#include <stdint.h>

typedef __attribute__((ext_vector_type(8))) short bf16x8;
typedef __attribute__((ext_vector_type(4))) short s16x4;
typedef __attribute__((ext_vector_type(4))) float f32x4;
typedef __attribute__((ext_vector_type(2))) float f32x2;
typedef __attribute__((ext_vector_type(16))) float f32x16;
typedef __attribute__((ext_vector_type(2))) __bf16 bf2_t;
typedef __attribute__((ext_vector_type(4))) unsigned u32x4;
typedef __attribute__((ext_vector_type(2))) unsigned u32x2;
typedef unsigned short u16;
typedef __attribute__((address_space(3))) s16x4 lds_s16x4;

#define DI __device__ __forceinline__

constexpr int SEQ = 2048, DM = 640, NH = 32, DH = 20, DF = 1728;

DI unsigned pack_bf2(float a, float b) {
  f32x2 v = {a, b};
  return __builtin_bit_cast(unsigned, __builtin_convertvector(v, bf2_t));
}
DI float bf_lo(unsigned u) { return __uint_as_float(u << 16); }
DI float bf_hi(unsigned u) { return __uint_as_float(u & 0xffff0000u); }
DI float rbf(float x) { return bf_lo(pack_bf2(x, 0.f)); }

DI void glds16(const void* g, unsigned lds_addr) {
  unsigned keep;
  asm volatile("s_mov_b32 %0, m0\n\ts_mov_b32 m0, %2\n\ts_nop 0\n\tglobal_load_lds_dwordx4 %1, off\n\ts_mov_b32 m0, %0"
               : "=&s"(keep) : "v"(g), "s"(lds_addr) : "memory");
}
DI void glds16s(const void* sbase, unsigned voff, unsigned lds_addr) {
  unsigned keep;
  asm volatile("s_mov_b32 %0, m0\n\ts_mov_b32 m0, %3\n\ts_nop 0\n\tglobal_load_lds_dwordx4 %1, %2\n\ts_mov_b32 m0, %0"
               : "=&s"(keep) : "v"(voff), "s"(sbase), "s"(lds_addr) : "memory");
}
DI void wait_vm0() { asm volatile("s_waitcnt vmcnt(0)" ::: "memory"); }
DI void barrier_raw() {
  asm volatile("s_waitcnt lgkmcnt(0)" ::: "memory");
  __builtin_amdgcn_s_barrier();
  asm volatile("" ::: "memory");
}
template <int V> struct IC { static constexpr int value = V; };
template <int N, int I = 0, class F> DI void static_for(F&& f) {
  if constexpr (I < N) { f(IC<I>{}); static_for<N, I + 1>(f); }
}
DI s16x4 tr_read(const char* lds) {
  return __builtin_amdgcn_ds_read_tr16_b64_v4i16((lds_s16x4*)lds);
}

__global__ __launch_bounds__(256) void ln_kernel(const u16* __restrict__ x, u16* __restrict__ xn) {
  const int row = blockIdx.x * 4 + (threadIdx.x >> 6), lane = threadIdx.x & 63;
  const unsigned* xr = (const unsigned*)(x + row * DM);
  float f[10];
#pragma unroll
  for (int j = 0; j < 5; ++j) {
    unsigned v = xr[lane + 64 * j];
    f[2 * j] = bf_lo(v);
    f[2 * j + 1] = bf_hi(v);
  }
  float s = 0.f;
#pragma unroll
  for (int j = 0; j < 10; ++j) s += f[j];
#pragma unroll
  for (int o = 32; o > 0; o >>= 1) s += __shfl_xor(s, o);
  const float mean = s * (1.f / DM);
  float q = 0.f;
#pragma unroll
  for (int j = 0; j < 10; ++j) { float d = f[j] - mean; q += d * d; }
#pragma unroll
  for (int o = 32; o > 0; o >>= 1) q += __shfl_xor(q, o);
  const float mb = rbf(mean);
  const float vb = rbf(q * (1.f / DM));
  const float den = rbf(sqrtf(rbf(vb + 1e-5f)));
  unsigned* o = (unsigned*)(xn + row * DM);
#pragma unroll
  for (int j = 0; j < 5; ++j)
    o[lane + 64 * j] = pack_bf2(rbf(f[2 * j] - mb) / den, rbf(f[2 * j + 1] - mb) / den);
}

constexpr int G1_BOFF = 3 * 32768;
__global__ __launch_bounds__(512, 2) void gemm1_kernel(const u16* __restrict__ xn, const u16* __restrict__ wqkv,
                                                       const u16* __restrict__ wg, const u16* __restrict__ wu,
                                                       u16* __restrict__ Qg, u16* __restrict__ Kf,
                                                       u16* __restrict__ Vf, u16* __restrict__ hbuf,
                                                       unsigned* __restrict__ cnt) {
  __shared__ __attribute__((aligned(16))) char smem[G1_BOFF + 2 * 24576];
  if (blockIdx.x == 0 && threadIdx.x < 8) cnt[threadIdx.x * 32] = 0u;
  const int b = blockIdx.x;
  const int t = (b & 7) * 28 + (b >> 3);
  const int nblk = t >> 3, mblk = t & 7;
  const int tid = threadIdx.x, wave = tid >> 6, lane = tid & 63;
  const int wm = wave >> 1, wn = wave & 1;
  const int m0 = mblk * 256;
  const bool isqkv = nblk < 10;

  const u16* a_src[4];
#pragma unroll
  for (int i = 0; i < 4; ++i) {
    int row = wave * 32 + i * 8 + (lane >> 3);
    int c = (lane & 7) ^ ((row >> 1) & 7);
    a_src[i] = xn + (size_t)(m0 + row) * DM + c * 8;
  }
  const int ldb = isqkv ? 3 * DM : DF;
  const u16* b_src[3];
#pragma unroll
  for (int i = 0; i < 3; ++i) {
    int kb = wave;
    int nt = i * 4 + (lane >> 4);
    int rr = ((lane >> 1) & 7) ^ ((kb & 1) << 2);
    int k = kb * 8 + rr;
    const u16* base;
    if (isqkv) base = wqkv + nblk * 192 + nt * 16;
    else base = (nt < 6) ? (wg + (nblk - 10) * 96 + nt * 16) : (wu + (nblk - 10) * 96 + (nt - 6) * 16);
    b_src[i] = base + (size_t)k * ldb + (lane & 1) * 8;
  }
  const unsigned lds0 = __builtin_amdgcn_readfirstlane((unsigned)(size_t)smem);
  const unsigned wv = __builtin_amdgcn_readfirstlane(wave);
  auto stage_a = [&](int kt, int g) { glds16(a_src[g] + kt * 64, lds0 + (kt % 3) * 32768 + (wv * 4 + g) * 1024); };
  auto stage_b = [&](int kt, int g) {
    glds16(b_src[g] + (size_t)kt * 64 * ldb, lds0 + G1_BOFF + (kt & 1) * 24576 + (wv * 3 + g) * 1024);
  };
#pragma unroll
  for (int kt = 0; kt < 2; ++kt) {
#pragma unroll
    for (int g = 0; g < 4; ++g) stage_a(kt, g);
#pragma unroll
    for (int g = 0; g < 3; ++g) stage_b(kt, g);
  }

  const int a_off = (wm * 64 + (lane & 15)) * 128 + (((lane >> 4) ^ ((lane >> 1) & 7)) << 4);
  int b_off[2];
  {
    int g = lane >> 4, q = (lane & 15) >> 2, p = lane & 3;
    int base = G1_BOFF + g * 3072 + ((g & 1) << 7) + q * 32 + p * 8;
    b_off[0] = base + (isqkv ? wn * 6 : wn * 3) * 256;
    b_off[1] = base + (isqkv ? wn * 6 + 3 : 6 + wn * 3) * 256;
  }

  f32x4 acc[4][6];
#pragma unroll
  for (int i = 0; i < 4; ++i)
#pragma unroll
    for (int j = 0; j < 6; ++j) acc[i][j] = (f32x4){0.f, 0.f, 0.f, 0.f};

  bf16x8 af0[4], bf0[6], af1[4], bf1[6];
  auto read_q = [&](bf16x8 (&af)[4], bf16x8 (&bfr)[6], int kt, int kk, int g) {
    const char* sa = smem + (kt % 3) * 32768;
    const char* sb = smem + (kt & 1) * 24576;
    auto rb = [&](int j) {
      s16x4 lo = tr_read(sb + b_off[j / 3] + (j % 3) * 256 + kk * 12288);
      s16x4 hi = tr_read(sb + (b_off[j / 3] ^ 128) + (j % 3) * 256 + kk * 12288);
      bfr[j] = __builtin_shufflevector(lo, hi, 0, 1, 2, 3, 4, 5, 6, 7);
    };
    auto ra = [&](int i) { af[i] = *(const bf16x8*)(sa + (a_off ^ (kk << 6)) + i * 2048); };
    if (g == 0) { ra(0); rb(0); rb(1); }
    if (g == 1) { rb(2); rb(3); }
    if (g == 2) { rb(4); rb(5); }
    if (g == 3) { ra(1); ra(2); ra(3); }
  };
  auto mma_q = [&](bf16x8 (&af)[4], bf16x8 (&bfr)[6], int i) {
#pragma unroll
    for (int j = 0; j < 6; ++j)
      acc[i][j] = __builtin_amdgcn_mfma_f32_16x16x32_bf16(bfr[j], af[i], acc[i][j], 0, 0, 0);
  };

  asm volatile("s_waitcnt vmcnt(7)" ::: "memory");
  barrier_raw();
#pragma unroll
  for (int g = 0; g < 4; ++g) read_q(af0, bf0, 0, 0, g);
#pragma unroll
  for (int kt = 0; kt < 10; ++kt) {
#pragma unroll
    for (int g = 0; g < 4; ++g) {
      __builtin_amdgcn_sched_barrier(0);
      read_q(af1, bf1, kt, 1, g);
      mma_q(af0, bf0, g);
      __builtin_amdgcn_sched_barrier(0);
      if (kt + 2 < 10) stage_a(kt + 2, g);
    }
    __builtin_amdgcn_sched_barrier(0);
    if (kt + 1 < 10) {
      if (kt + 2 < 10) asm volatile("s_waitcnt vmcnt(4)" ::: "memory");
      else wait_vm0();
      barrier_raw();
    }
#pragma unroll
    for (int g = 0; g < 4; ++g) {
      __builtin_amdgcn_sched_barrier(0);
      if (kt + 1 < 10) read_q(af0, bf0, kt + 1, 0, g);
      mma_q(af1, bf1, g);
      __builtin_amdgcn_sched_barrier(0);
      if (kt + 2 < 10 && g < 3) stage_b(kt + 2, g);
    }
  }
  __builtin_amdgcn_sched_barrier(0);
  barrier_raw();

  int tz = threadIdx.x;
  asm volatile("" : "+v"(tz));
  const int e_wave = tz >> 6, e_lane = tz & 63, e_wm = e_wave >> 1, e_wn = e_wave & 1;
  const int ln4 = (e_lane >> 4) * 4;
  if (isqkv) {
    const float qscale = 0.22360679774997896f * 1.4426950408889634f;
    char* wtr = smem + e_wave * 512;
#pragma unroll
    for (int j = 0; j < 6; ++j) {
      const int nb = nblk * 192 + (e_wn * 6 + j) * 16;
      const int sec = nb / DM;
      if (sec < 2) {
        const int rr = nb + ln4 - sec * DM, head = rr / DH, d = rr - head * DH, d4 = d >> 2;
#pragma unroll
        for (int i = 0; i < 4; ++i) {
          const int tok = m0 + e_wm * 64 + i * 16 + (e_lane & 15);
          f32x4 v = acc[i][j];
          if (sec == 0) {
            u32x2 o = {pack_bf2(v[0] * qscale, v[1] * qscale), pack_bf2(v[2] * qscale, v[3] * qscale)};
            *(u32x2*)(Qg + ((size_t)(head * SEQ + tok) * 32 + d)) = o;
          } else {
            char* kb = (char*)Kf + (size_t)(head * 64 + (tok >> 5)) * 1536;
            const int r = tok & 31;
            unsigned p0 = pack_bf2(v[0], v[1]), p1 = pack_bf2(v[2], v[3]);
            if (d4 < 4) {
              u32x2 o = {p0, p1};
              *(u32x2*)(kb + ((d4 >> 1) * 32 + r) * 16 + (d4 & 1) * 8) = o;
            } else {
              u32x4 o = {p0, p1, 0x00003F80u, 0u};
              *(u32x4*)(kb + 1024 + r * 16) = o;
            }
          }
        }
      } else {
        const int rr = nb + (e_lane & 15) - 2 * DM, head = rr / DH, d = rr - head * DH;
        const int g = e_lane >> 4;
#pragma unroll
        for (int i = 0; i < 4; ++i) {
          f32x4 v = acc[i][j];
          u32x2 w = {pack_bf2(v[0], v[1]), pack_bf2(v[2], v[3])};
          *(u32x2*)(wtr + (e_lane & 15) * 32 + g * 8) = w;
          s16x4 tv = tr_read(wtr + g * 128 + ((e_lane & 15) >> 2) * 32 + (e_lane & 3) * 8);
          const int s = (m0 + e_wm * 64 + i * 16) >> 4;
          u16* vb = Vf + ((size_t)(head * 128 + s) * 48 + (g & 1) * 24 + d) * 8 + (g >> 1) * 4;
          *(s16x4*)vb = tv;
          if (d == DH - 1) { s16x4 one = {0x3F80, 0x3F80, 0x3F80, 0x3F80}; *(s16x4*)(vb + 8) = one; }
        }
      }
    }
  } else {
#pragma unroll
    for (int j = 0; j < 3; ++j) {
      const int f = (nblk - 10) * 96 + (e_wn * 3 + j) * 16 + ln4;
#pragma unroll
      for (int i = 0; i < 4; ++i) {
        const int tok = m0 + e_wm * 64 + i * 16 + (e_lane & 15);
        float hv[4];
#pragma unroll
        for (int r = 0; r < 4; ++r) {
          const float g = acc[i][j][r], u = acc[i][j + 3][r];
          const float e = __builtin_amdgcn_exp2f(g * __builtin_fmaf(g * g, -0.10294324f, -2.30220819f));
          hv[r] = (g * u) * __builtin_amdgcn_rcpf(1.f + e);
        }
        u32x2 o = {pack_bf2(hv[0], hv[1]), pack_bf2(hv[2], hv[3])};
        *(u32x2*)(hbuf + (size_t)tok * DF + f) = o;
      }
    }
  }
}

constexpr int AT_STAGE = 24576;
constexpr float AT_BIAS = 8.f;
#define MFMA32(a, b, c) __builtin_amdgcn_mfma_f32_32x32x16_bf16((a), (b), (c), 0, 0, 0)
#define MFMA16(a, b, c) __builtin_amdgcn_mfma_f32_16x16x32_bf16((a), (b), (c), 0, 0, 0)

typedef __attribute__((address_space(1))) unsigned gu32;
typedef __attribute__((address_space(1))) unsigned long long gu64;
DI void store8_agent(u16* p, u32x2 v) {
  __hip_atomic_store((gu64*)p, ((unsigned long long)v[1] << 32) | v[0], __ATOMIC_RELAXED, __HIP_MEMORY_SCOPE_AGENT);
}

DI void attn_role(char* smem, int b, const u16* __restrict__ Qg, const u16* __restrict__ Kf,
                  const u16* __restrict__ Vf, u16* __restrict__ obuf, unsigned* __restrict__ cnt) {
  const int head = (b & 7) * 4 + ((b >> 3) & 3), qb = b >> 5;
  const int tid = threadIdx.x, wave = tid >> 6, lane = tid & 63;
  const int r = lane & 31, h = lane >> 5;
  const int q = qb * 256 + wave * 32 + r;

  const unsigned lds0 = __builtin_amdgcn_readfirstlane((unsigned)(size_t)smem);
  const unsigned wv = __builtin_amdgcn_readfirstlane(wave);
  const char* gsrc = ((wv < 4) ? (const char*)Kf + wv * 3072 : (const char*)Vf + (wv - 4) * 3072) +
                     (size_t)head * 98304 + lane * 16;
  auto stage = [&](unsigned boff, int tile) {
#pragma unroll
    for (int i = 0; i < 3; ++i) glds16(gsrc + tile * 12288 + i * 1024, lds0 + boff + (wv * 3 + i) * 1024);
  };
  const u16* qrow = Qg + (size_t)(head * SEQ + q) * 32;
  bf16x8 q1 = *(const bf16x8*)(qrow + 8 * h);
  u32x4 q2 = {0u, 0u, 0u, 0u};
  {
    u32x2 t2 = *(const u32x2*)(qrow + 16);
    if (h == 0) { q2[0] = t2[0]; q2[1] = t2[1]; }
  }
  stage(0, 0);
  stage(AT_STAGE, 1);

  const int k1_off = lane * 16, k2_off = 1024 + r * 16;
  const int v_off = 12288 + (h * 24 + (r < 23 ? r : 23)) * 16;

  f32x16 o, s0, s1;
  const f32x16 zero = {0.f, 0.f, 0.f, 0.f, 0.f, 0.f, 0.f, 0.f, 0.f, 0.f, 0.f, 0.f, 0.f, 0.f, 0.f, 0.f};
  o = zero;
  float mref;

  asm volatile("s_waitcnt vmcnt(3)" ::: "memory");
  barrier_raw();
  {
    s0 = MFMA32(*(const bf16x8*)(smem + k1_off), q1, zero);
    s0 = MFMA32(*(const bf16x8*)(smem + k2_off), __builtin_bit_cast(bf16x8, q2), s0);
    s1 = MFMA32(*(const bf16x8*)(smem + 1536 + k1_off), q1, zero);
    s1 = MFMA32(*(const bf16x8*)(smem + 1536 + k2_off), __builtin_bit_cast(bf16x8, q2), s1);
    float mx = fmaxf(s0[0], s1[0]);
#pragma unroll
    for (int i = 1; i < 16; ++i) mx = fmaxf(mx, fmaxf(s0[i], s1[i]));
    mref = rbf(fmaxf(mx, __shfl_xor(mx, 32)) + AT_BIAS);
    if (h == 0) q2[2] = pack_bf2(-mref, 0.f);
#pragma unroll
    for (int i = 0; i < 16; ++i) { s0[i] -= mref; s1[i] -= mref; }
  }
  unsigned cur = 0;
  for (int tile = 0; tile < 8; ++tile) {
    const unsigned nxt = (cur == 2 * AT_STAGE) ? 0 : cur + AT_STAGE;
#pragma unroll
    for (int g = 0; g < 4; ++g) {
      f32x16 n0, n1;
      const char* kb;
      if (g == 3) {
        if (tile < 7) {
          wait_vm0();
          barrier_raw();
          if (tile < 6) stage((nxt == 2 * AT_STAGE) ? 0 : nxt + AT_STAGE, tile + 2);
        }
        kb = smem + nxt;
      } else {
        kb = smem + cur + (g + 1) * 3072;
      }
      n0 = MFMA32(*(const bf16x8*)(kb + k1_off), q1, zero);
      n0 = MFMA32(*(const bf16x8*)(kb + k2_off), __builtin_bit_cast(bf16x8, q2), n0);
      n1 = MFMA32(*(const bf16x8*)(kb + 1536 + k1_off), q1, zero);
      n1 = MFMA32(*(const bf16x8*)(kb + 1536 + k2_off), __builtin_bit_cast(bf16x8, q2), n1);
      unsigned p[16];
#pragma unroll
      for (int i = 0; i < 8; ++i) {
        p[i] = pack_bf2(__builtin_amdgcn_exp2f(s0[2 * i]), __builtin_amdgcn_exp2f(s0[2 * i + 1]));
        p[8 + i] = pack_bf2(__builtin_amdgcn_exp2f(s1[2 * i]), __builtin_amdgcn_exp2f(s1[2 * i + 1]));
      }
      unsigned orr = p[0];
#pragma unroll
      for (int i = 1; i < 16; ++i) orr |= p[i];
      if (__builtin_amdgcn_ballot_w64((orr & 0x40004000u) != 0) != 0) {
        const char* kc = smem + cur + g * 3072;
        float mx = -3.0e38f;
#pragma unroll
        for (int hf = 0; hf < 2; ++hf) {
          f32x16 t = MFMA32(*(const bf16x8*)(kc + hf * 1536 + k1_off), q1, zero);
          t = MFMA32(*(const bf16x8*)(kc + hf * 1536 + k2_off), __builtin_bit_cast(bf16x8, q2), t);
#pragma unroll
          for (int i = 0; i < 16; ++i) mx = fmaxf(mx, t[i]);
          __builtin_amdgcn_sched_barrier(0);
        }
        mx = fmaxf(mx, __shfl_xor(mx, 32));
        const float mnew = rbf(mref + fmaxf(mx + AT_BIAS, 0.f));
        const float delta = mnew - mref;
        const float alpha = __builtin_amdgcn_exp2f(-delta);
        mref = mnew;
        if (h == 0) q2[2] = pack_bf2(-mref, 0.f);
#pragma unroll
        for (int i = 0; i < 16; ++i) { n0[i] -= delta; n1[i] -= delta; o[i] *= alpha; }
#pragma unroll
        for (int hf = 0; hf < 2; ++hf) {
          __builtin_amdgcn_sched_barrier(0);
          f32x16 t = MFMA32(*(const bf16x8*)(kc + hf * 1536 + k1_off), q1, zero);
          t = MFMA32(*(const bf16x8*)(kc + hf * 1536 + k2_off), __builtin_bit_cast(bf16x8, q2), t);
#pragma unroll
          for (int i = 0; i < 8; ++i)
            p[hf * 8 + i] = pack_bf2(__builtin_amdgcn_exp2f(t[2 * i]), __builtin_amdgcn_exp2f(t[2 * i + 1]));
        }
      }
#pragma unroll
      for (int s = 0; s < 4; ++s) {
        u32x4 pv = {p[4 * s], p[4 * s + 1], p[4 * s + 2], p[4 * s + 3]};
        bf16x8 vf = *(const bf16x8*)(smem + cur + (g * 4 + s) * 768 + v_off);
        o = MFMA32(vf, __builtin_bit_cast(bf16x8, pv), o);
      }
      s0 = n0; s1 = n1;
    }
    cur = nxt;
  }
  const float l = __shfl(o[8], r + 32);
  const float inv = 1.f / l;
  u16* orow = obuf + (size_t)q * DM + head * DH + 4 * h;
  {
    u32x2 w0 = {pack_bf2(o[0] * inv, o[1] * inv), pack_bf2(o[2] * inv, o[3] * inv)};
    u32x2 w1 = {pack_bf2(o[4] * inv, o[5] * inv), pack_bf2(o[6] * inv, o[7] * inv)};
    store8_agent(orow, w0);
    store8_agent(orow + 8, w1);
    if (h == 0) {
      u32x2 w2 = {pack_bf2(o[8] * inv, o[9] * inv), pack_bf2(o[10] * inv, o[11] * inv)};
      store8_agent(orow + 16, w2);
    }
  }
  wait_vm0();
  barrier_raw();
  if (tid == 0) __hip_atomic_fetch_add((gu32*)(cnt + qb * 32), 1u, __ATOMIC_RELAXED, __HIP_MEMORY_SCOPE_AGENT);
}

constexpr int GS_STAGE = 18432;
DI void out_role(char* smem, int b, const u16* __restrict__ hbuf, const u16* __restrict__ wd,
                 const u16* __restrict__ obuf, const u16* __restrict__ wo, const u16* __restrict__ x,
                 u16* __restrict__ out, unsigned* __restrict__ cnt) {
  constexpr int NSLOT = 4, NH_ST = DF / 64, NST = NH_ST + DM / 64;
  const int t = (b & 7) * 32 + (b >> 3);
  const int nblk = t & 7, mblk = t >> 3;
  const int tid = threadIdx.x, wave = tid >> 6, lane = tid & 63;
  const int m0 = mblk * 64, n0 = nblk * 80;
  const unsigned lds0 = __builtin_amdgcn_readfirstlane((unsigned)(size_t)smem);
  const unsigned wv = __builtin_amdgcn_readfirstlane(wave);

  unsigned ah_vo, ao_vo, b_vo;
  {
    int row = wave * 8 + (lane >> 3);
    int c8 = ((lane & 7) ^ ((row >> 1) & 7)) * 8;
    ah_vo = ((m0 + row) * DF + c8) * 2;
    ao_vo = ((m0 + row) * DM + c8) * 2;
    b_vo = ((wave * 8 + (((lane >> 1) & 7) ^ ((wave & 1) << 2))) * DM + n0 + (lane >> 4) * 16 + (lane & 1) * 8) * 2;
  }
  auto stage = [&](int st) {
    unsigned sa = lds0 + (st & (NSLOT - 1)) * GS_STAGE;
    const u16* B;
    if (st < NH_ST) { glds16s(hbuf + st * 64, ah_vo, sa + wv * 1024); B = wd + (size_t)st * 64 * DM; }
    else { glds16s(obuf + (st - NH_ST) * 64, ao_vo, sa + wv * 1024); B = wo + (size_t)(st - NH_ST) * 64 * DM; }
    glds16s(B, b_vo, sa + 8192 + wv * 1280);
    if (lane < 16) glds16s(B + 64, b_vo, sa + 8192 + wv * 1280 + 1024);
  };
#pragma unroll
  for (int st = 0; st < NSLOT - 1; ++st) stage(st);
  const int mh = wave & 1, kk = (wave >> 1) & 1;
  const unsigned par = wv >> 2;
  const int a_off = (mh * 32 + (lane & 15)) * 128 + (((kk * 4 + (lane >> 4)) ^ ((lane >> 1) & 7)) << 4);
  int b_off;
  {
    int g = lane >> 4, q = (lane & 15) >> 2, p = lane & 3;
    b_off = 8192 + (kk * 4 + g) * 1280 + ((g & 1) << 7) + q * 32 + p * 8;
  }
  f32x4 acc[2][5];
#pragma unroll
  for (int i = 0; i < 2; ++i)
#pragma unroll
    for (int j = 0; j < 5; ++j) acc[i][j] = (f32x4){0.f, 0.f, 0.f, 0.f};

  auto step = [&](int st, auto waitc) {
    asm volatile("s_waitcnt vmcnt(%0)" :: "i"(decltype(waitc)::value) : "memory");
    barrier_raw();
    if (st + NSLOT - 1 < NST) stage(st + NSLOT - 1);
    if ((unsigned)(st & 1) == par) {
      const char* sbase = smem + (st & (NSLOT - 1)) * GS_STAGE;
      bf16x8 af0 = *(const bf16x8*)(sbase + a_off);
      bf16x8 af1 = *(const bf16x8*)(sbase + a_off + 2048);
      bf16x8 bfr[5];
#pragma unroll
      for (int j = 0; j < 5; ++j) {
        s16x4 lo = tr_read(sbase + b_off + j * 256);
        s16x4 hi = tr_read(sbase + (b_off ^ 128) + j * 256);
        bfr[j] = __builtin_shufflevector(lo, hi, 0, 1, 2, 3, 4, 5, 6, 7);
      }
#pragma unroll
      for (int j = 0; j < 5; ++j) {
        acc[0][j] = MFMA16(bfr[j], af0, acc[0][j]);
        acc[1][j] = MFMA16(bfr[j], af1, acc[1][j]);
      }
    }
  };
  for (int st = 0; st < NH_ST - (NSLOT - 1); ++st) step(st, IC<3 * (NSLOT - 2)>{});
  if (tid == 0) {
    gu32* c = (gu32*)(cnt + (mblk >> 2) * 32);
    for (int spin = 0; spin < (1 << 22); ++spin) {
      if (__hip_atomic_load(c, __ATOMIC_RELAXED, __HIP_MEMORY_SCOPE_AGENT) >= (unsigned)NH) break;
      __builtin_amdgcn_s_sleep(8);
    }
    __builtin_amdgcn_fence(__ATOMIC_ACQUIRE, "agent");
    wait_vm0();
  }
  barrier_raw();
  for (int st = NH_ST - (NSLOT - 1); st < NST - (NSLOT - 2); ++st) step(st, IC<3 * (NSLOT - 2)>{});
  static_for<NSLOT - 2>([&](auto i) { step(NST - (NSLOT - 2) + i.value, IC<3 * (NSLOT - 3 - i.value)>{}); });
  barrier_raw();
  if (wv >= 2) {
    float* red = (float*)smem + ((wave - 2) * 64 + lane) * 40;
#pragma unroll
    for (int i = 0; i < 2; ++i)
#pragma unroll
      for (int j = 0; j < 5; ++j) *(f32x4*)(red + (i * 5 + j) * 4) = acc[i][j];
  }
  barrier_raw();
  if (wv < 2) {
#pragma unroll
    for (int i = 0; i < 2; ++i) {
      const size_t gi = (size_t)(m0 + mh * 32 + i * 16 + (lane & 15)) * DM + n0 + (lane >> 4) * 4;
#pragma unroll
      for (int j = 0; j < 5; ++j) {
        f32x4 s = acc[i][j];
#pragma unroll
        for (int w = 0; w < 3; ++w) s += *(const f32x4*)((float*)smem + ((w * 2 + mh) * 64 + lane) * 40 + (i * 5 + j) * 4);
        u32x2 xv = *(const u32x2*)(x + gi + j * 16);
        u32x2 o = {pack_bf2(s[0] + bf_lo(xv[0]), s[1] + bf_hi(xv[0])), pack_bf2(s[2] + bf_lo(xv[1]), s[3] + bf_hi(xv[1]))};
        *(u32x2*)(out + gi + j * 16) = o;
      }
    }
  }
}

__global__ __launch_bounds__(512, 4) void k2_kernel(const u16* __restrict__ Qg, const u16* __restrict__ Kf,
                                                    const u16* __restrict__ Vf, u16* __restrict__ obuf,
                                                    const u16* __restrict__ hbuf, const u16* __restrict__ wd,
                                                    const u16* __restrict__ wo, const u16* __restrict__ x,
                                                    u16* __restrict__ out, unsigned* __restrict__ cnt) {
  __shared__ __attribute__((aligned(16))) char smem[3 * AT_STAGE];
  const int b = blockIdx.x;
  if (b < 256) attn_role(smem, b, Qg, Kf, Vf, obuf, cnt);
  else out_role(smem, b - 256, hbuf, wd, obuf, wo, x, out, cnt);
}

extern "C" void kernel_launch(void* const* d_in, const int* in_sizes, int n_in, void* d_out, int out_size,
                              void* d_ws, size_t ws_size, hipStream_t stream) {
  const u16* x = (const u16*)d_in[0];
  const u16* wqkv = (const u16*)d_in[1];
  const u16* wo = (const u16*)d_in[2];
  const u16* wg = (const u16*)d_in[4];
  const u16* wu = (const u16*)d_in[5];
  const u16* wd = (const u16*)d_in[6];
  char* ws = (char*)d_ws;
  u16* xn = (u16*)(ws);
  u16* Qg = (u16*)(ws + (4u << 20));
  u16* Kf = (u16*)(ws + (8u << 20));
  u16* Vf = (u16*)(ws + (12u << 20));
  u16* hb = (u16*)(ws + (16u << 20));
  u16* ob = (u16*)(ws + (24u << 20));
  unsigned* cnt = (unsigned*)(ws + (28u << 20));
  ln_kernel<<<SEQ / 4, 256, 0, stream>>>(x, xn);
  gemm1_kernel<<<224, 512, 0, stream>>>(xn, wqkv, wg, wu, Qg, Kf, Vf, hb, cnt);
  k2_kernel<<<512, 512, 0, stream>>>(Qg, Kf, Vf, ob, hb, wd, wo, x, (u16*)d_out, cnt);
}
```

```cpp
#include <hip/hip_runtime.h>
#include <stdint.h>

typedef __attribute__((ext_vector_type(8))) short bf16x8;
typedef __attribute__((ext_vector_type(4))) short s16x4;
typedef __attribute__((ext_vector_type(4))) float f32x4;
typedef __attribute__((ext_vector_type(2))) float f32x2;
typedef __attribute__((ext_vector_type(16))) float f32x16;
typedef __attribute__((ext_vector_type(2))) __bf16 bf2_t;
typedef __attribute__((ext_vector_type(4))) unsigned u32x4;
typedef __attribute__((ext_vector_type(2))) unsigned u32x2;
typedef unsigned short u16;
typedef __attribute__((address_space(3))) s16x4 lds_s16x4;

#define DI __device__ __forceinline__

constexpr int SEQ = 2048, DM = 640, NH = 32, DH = 20, DF = 1728;

DI unsigned pack_bf2(float a, float b) {
  f32x2 v = {a, b};
  return __builtin_bit_cast(unsigned, __builtin_convertvector(v, bf2_t));
}
DI float bf_lo(unsigned u) { return __uint_as_float(u << 16); }
DI float bf_hi(unsigned u) { return __uint_as_float(u & 0xffff0000u); }
DI float rbf(float x) { return bf_lo(pack_bf2(x, 0.f)); }

DI void glds16(const void* g, unsigned lds_addr) {
  unsigned keep;
  asm volatile("s_mov_b32 %0, m0\n\ts_mov_b32 m0, %2\n\ts_nop 0\n\tglobal_load_lds_dwordx4 %1, off\n\ts_mov_b32 m0, %0"
               : "=&s"(keep) : "v"(g), "s"(lds_addr) : "memory");
}
DI void glds16s(const void* sbase, unsigned voff, unsigned lds_addr) {
  unsigned keep;
  asm volatile("s_mov_b32 %0, m0\n\ts_mov_b32 m0, %3\n\ts_nop 0\n\tglobal_load_lds_dwordx4 %1, %2\n\ts_mov_b32 m0, %0"
               : "=&s"(keep) : "v"(voff), "s"(sbase), "s"(lds_addr) : "memory");
}
DI void wait_vm0() { asm volatile("s_waitcnt vmcnt(0)" ::: "memory"); }
DI void barrier_raw() {
  asm volatile("s_waitcnt lgkmcnt(0)" ::: "memory");
  __builtin_amdgcn_s_barrier();
  asm volatile("" ::: "memory");
}
template <int V> struct IC { static constexpr int value = V; };
template <int N, int I = 0, class F> DI void static_for(F&& f) {
  if constexpr (I < N) { f(IC<I>{}); static_for<N, I + 1>(f); }
}
DI s16x4 tr_read(const char* lds) {
  return __builtin_amdgcn_ds_read_tr16_b64_v4i16((lds_s16x4*)lds);
}

__global__ __launch_bounds__(256) void ln_kernel(const u16* __restrict__ x, u16* __restrict__ xn) {
  const int row = blockIdx.x * 4 + (threadIdx.x >> 6), lane = threadIdx.x & 63;
  const unsigned* xr = (const unsigned*)(x + row * DM);
  float f[10];
#pragma unroll
  for (int j = 0; j < 5; ++j) {
    unsigned v = xr[lane + 64 * j];
    f[2 * j] = bf_lo(v);
    f[2 * j + 1] = bf_hi(v);
  }
  float s = 0.f;
#pragma unroll
  for (int j = 0; j < 10; ++j) s += f[j];
#pragma unroll
  for (int o = 32; o > 0; o >>= 1) s += __shfl_xor(s, o);
  const float mean = s * (1.f / DM);
  float q = 0.f;
#pragma unroll
  for (int j = 0; j < 10; ++j) { float d = f[j] - mean; q += d * d; }
#pragma unroll
  for (int o = 32; o > 0; o >>= 1) q += __shfl_xor(q, o);
  const float mb = rbf(mean);
  const float vb = rbf(q * (1.f / DM));
  const float den = rbf(sqrtf(rbf(vb + 1e-5f)));
  unsigned* o = (unsigned*)(xn + row * DM);
#pragma unroll
  for (int j = 0; j < 5; ++j)
    o[lane + 64 * j] = pack_bf2(rbf(f[2 * j] - mb) / den, rbf(f[2 * j + 1] - mb) / den);
}

constexpr int G1_BOFF = 3 * 32768;
__global__ __launch_bounds__(512, 2) void gemm1_kernel(const u16* __restrict__ xn, const u16* __restrict__ wqkv,
                                                       const u16* __restrict__ wg, const u16* __restrict__ wu,
                                                       u16* __restrict__ Qg, u16* __restrict__ Kf,
                                                       u16* __restrict__ Vf, u16* __restrict__ hbuf) {
  __shared__ __attribute__((aligned(16))) char smem[G1_BOFF + 2 * 24576];
  const int b = blockIdx.x;
  const int t = (b & 7) * 28 + (b >> 3);
  const int nblk = t >> 3, mblk = t & 7;
  const int tid = threadIdx.x, wave = tid >> 6, lane = tid & 63;
  const int wm = wave >> 1, wn = wave & 1;
  const int m0 = mblk * 256;
  const bool isqkv = nblk < 10;

  const u16* a_src[4];
#pragma unroll
  for (int i = 0; i < 4; ++i) {
    int row = wave * 32 + i * 8 + (lane >> 3);
    int c = (lane & 7) ^ ((row >> 1) & 7);
    a_src[i] = xn + (size_t)(m0 + row) * DM + c * 8;
  }
  const int ldb = isqkv ? 3 * DM : DF;
  const u16* b_src[3];
#pragma unroll
  for (int i = 0; i < 3; ++i) {
    int kb = wave;
    int nt = i * 4 + (lane >> 4);
    int rr = ((lane >> 1) & 7) ^ ((kb & 1) << 2);
    int k = kb * 8 + rr;
    const u16* base;
    if (isqkv) base = wqkv + nblk * 192 + nt * 16;
    else base = (nt < 6) ? (wg + (nblk - 10) * 96 + nt * 16) : (wu + (nblk - 10) * 96 + (nt - 6) * 16);
    b_src[i] = base + (size_t)k * ldb + (lane & 1) * 8;
  }
  const unsigned lds0 = __builtin_amdgcn_readfirstlane((unsigned)(size_t)smem);
  const unsigned wv = __builtin_amdgcn_readfirstlane(wave);
  auto stage_a = [&](int kt, int g) { glds16(a_src[g] + kt * 64, lds0 + (kt % 3) * 32768 + (wv * 4 + g) * 1024); };
  auto stage_b = [&](int kt, int g) {
    glds16(b_src[g] + (size_t)kt * 64 * ldb, lds0 + G1_BOFF + (kt & 1) * 24576 + (wv * 3 + g) * 1024);
  };
#pragma unroll
  for (int kt = 0; kt < 2; ++kt) {
#pragma unroll
    for (int g = 0; g < 4; ++g) stage_a(kt, g);
#pragma unroll
    for (int g = 0; g < 3; ++g) stage_b(kt, g);
  }

  const int a_off = (wm * 64 + (lane & 15)) * 128 + (((lane >> 4) ^ ((lane >> 1) & 7)) << 4);
  int b_off[2];
  {
    int g = lane >> 4, q = (lane & 15) >> 2, p = lane & 3;
    int base = G1_BOFF + g * 3072 + ((g & 1) << 7) + q * 32 + p * 8;
    b_off[0] = base + (isqkv ? wn * 6 : wn * 3) * 256;
    b_off[1] = base + (isqkv ? wn * 6 + 3 : 6 + wn * 3) * 256;
  }

  f32x4 acc[4][6];
#pragma unroll
  for (int i = 0; i < 4; ++i)
#pragma unroll
    for (int j = 0; j < 6; ++j) acc[i][j] = (f32x4){0.f, 0.f, 0.f, 0.f};

  bf16x8 af0[4], bf0[6], af1[4], bf1[6];
  auto read_q = [&](bf16x8 (&af)[4], bf16x8 (&bfr)[6], int kt, int kk, int g) {
    const char* sa = smem + (kt % 3) * 32768;
    const char* sb = smem + (kt & 1) * 24576;
    auto rb = [&](int j) {
      s16x4 lo = tr_read(sb + b_off[j / 3] + (j % 3) * 256 + kk * 12288);
      s16x4 hi = tr_read(sb + (b_off[j / 3] ^ 128) + (j % 3) * 256 + kk * 12288);
      bfr[j] = __builtin_shufflevector(lo, hi, 0, 1, 2, 3, 4, 5, 6, 7);
    };
    auto ra = [&](int i) { af[i] = *(const bf16x8*)(sa + (a_off ^ (kk << 6)) + i * 2048); };
    if (g == 0) { ra(0); rb(0); rb(1); }
    if (g == 1) { rb(2); rb(3); }
    if (g == 2) { rb(4); rb(5); }
    if (g == 3) { ra(1); ra(2); ra(3); }
  };
  auto mma_q = [&](bf16x8 (&af)[4], bf16x8 (&bfr)[6], int i) {
#pragma unroll
    for (int j = 0; j < 6; ++j)
      acc[i][j] = __builtin_amdgcn_mfma_f32_16x16x32_bf16(bfr[j], af[i], acc[i][j], 0, 0, 0);
  };

  asm volatile("s_waitcnt vmcnt(7)" ::: "memory");
  barrier_raw();
#pragma unroll
  for (int g = 0; g < 4; ++g) read_q(af0, bf0, 0, 0, g);
#pragma unroll
  for (int kt = 0; kt < 10; ++kt) {
#pragma unroll
    for (int g = 0; g < 4; ++g) {
      __builtin_amdgcn_sched_barrier(0);
      read_q(af1, bf1, kt, 1, g);
      mma_q(af0, bf0, g);
      __builtin_amdgcn_sched_barrier(0);
      if (kt + 2 < 10) stage_a(kt + 2, g);
    }
    __builtin_amdgcn_sched_barrier(0);
    if (kt + 1 < 10) {
      if (kt + 2 < 10) asm volatile("s_waitcnt vmcnt(4)" ::: "memory");
      else wait_vm0();
      barrier_raw();
    }
#pragma unroll
    for (int g = 0; g < 4; ++g) {
      __builtin_amdgcn_sched_barrier(0);
      if (kt + 1 < 10) read_q(af0, bf0, kt + 1, 0, g);
      mma_q(af1, bf1, g);
      __builtin_amdgcn_sched_barrier(0);
      if (kt + 2 < 10 && g < 3) stage_b(kt + 2, g);
    }
  }
  __builtin_amdgcn_sched_barrier(0);
  barrier_raw();

  int tz = threadIdx.x;
  asm volatile("" : "+v"(tz));
  const int e_wave = tz >> 6, e_lane = tz & 63, e_wm = e_wave >> 1, e_wn = e_wave & 1;
  const int ln4 = (e_lane >> 4) * 4;
  if (isqkv) {
    const float qscale = 0.22360679774997896f * 1.4426950408889634f;
    char* wtr = smem + e_wave * 512;
#pragma unroll
    for (int j = 0; j < 6; ++j) {
      const int nb = nblk * 192 + (e_wn * 6 + j) * 16;
      const int sec = nb / DM;
      if (sec < 2) {
        const int rr = nb + ln4 - sec * DM, head = rr / DH, d = rr - head * DH, d4 = d >> 2;
#pragma unroll
        for (int i = 0; i < 4; ++i) {
          const int tok = m0 + e_wm * 64 + i * 16 + (e_lane & 15);
          f32x4 v = acc[i][j];
          if (sec == 0) {
            u32x2 o = {pack_bf2(v[0] * qscale, v[1] * qscale), pack_bf2(v[2] * qscale, v[3] * qscale)};
            *(u32x2*)(Qg + ((size_t)(head * SEQ + tok) * 32 + d)) = o;
          } else {
            char* kb = (char*)Kf + (size_t)(head * 64 + (tok >> 5)) * 1536;
            const int r = tok & 31;
            unsigned p0 = pack_bf2(v[0], v[1]), p1 = pack_bf2(v[2], v[3]);
            if (d4 < 4) {
              u32x2 o = {p0, p1};
              *(u32x2*)(kb + ((d4 >> 1) * 32 + r) * 16 + (d4 & 1) * 8) = o;
            } else {
              u32x4 o = {p0, p1, 0x00003F80u, 0u};
              *(u32x4*)(kb + 1024 + r * 16) = o;
            }
          }
        }
      } else {
        const int rr = nb + (e_lane & 15) - 2 * DM, head = rr / DH, d = rr - head * DH;
        const int g = e_lane >> 4;
#pragma unroll
        for (int i = 0; i < 4; ++i) {
          f32x4 v = acc[i][j];
          u32x2 w = {pack_bf2(v[0], v[1]), pack_bf2(v[2], v[3])};
          *(u32x2*)(wtr + (e_lane & 15) * 32 + g * 8) = w;
          s16x4 tv = tr_read(wtr + g * 128 + ((e_lane & 15) >> 2) * 32 + (e_lane & 3) * 8);
          const int s = (m0 + e_wm * 64 + i * 16) >> 4;
          u16* vb = Vf + ((size_t)(head * 128 + s) * 48 + (g & 1) * 24 + d) * 8 + (g >> 1) * 4;
          *(s16x4*)vb = tv;
          if (d == DH - 1) { s16x4 one = {0x3F80, 0x3F80, 0x3F80, 0x3F80}; *(s16x4*)(vb + 8) = one; }
        }
      }
    }
  } else {
#pragma unroll
    for (int j = 0; j < 3; ++j) {
      const int f = (nblk - 10) * 96 + (e_wn * 3 + j) * 16 + ln4;
#pragma unroll
      for (int i = 0; i < 4; ++i) {
        const int tok = m0 + e_wm * 64 + i * 16 + (e_lane & 15);
        float hv[4];
#pragma unroll
        for (int r = 0; r < 4; ++r) {
          const float g = acc[i][j][r], u = acc[i][j + 3][r];
          const float e = __builtin_amdgcn_exp2f(g * __builtin_fmaf(g * g, -0.10294324f, -2.30220819f));
          hv[r] = (g * u) * __builtin_amdgcn_rcpf(1.f + e);
        }
        u32x2 o = {pack_bf2(hv[0], hv[1]), pack_bf2(hv[2], hv[3])};
        *(u32x2*)(hbuf + (size_t)tok * DF + f) = o;
      }
    }
  }
}

constexpr int AT_STAGE = 24576;
constexpr float AT_BIAS = 8.f;
#define MFMA32(a, b, c) __builtin_amdgcn_mfma_f32_32x32x16_bf16((a), (b), (c), 0, 0, 0)
#define MFMA16(a, b, c) __builtin_amdgcn_mfma_f32_16x16x32_bf16((a), (b), (c), 0, 0, 0)

DI void attn_role(char* smem, int b, const u16* __restrict__ Qg, const u16* __restrict__ Kf,
                  const u16* __restrict__ Vf, u16* __restrict__ obuf) {
  const int head = (b & 7) * 4 + ((b >> 3) & 3), qb = b >> 5;
  const int tid = threadIdx.x, wave = tid >> 6, lane = tid & 63;
  const int r = lane & 31, h = lane >> 5;
  const int q = qb * 256 + wave * 32 + r;

  const unsigned lds0 = __builtin_amdgcn_readfirstlane((unsigned)(size_t)smem);
  const unsigned wv = __builtin_amdgcn_readfirstlane(wave);
  const char* gsrc = ((wv < 4) ? (const char*)Kf + wv * 3072 : (const char*)Vf + (wv - 4) * 3072) +
                     (size_t)head * 98304 + lane * 16;
  auto stage = [&](unsigned boff, int tile) {
#pragma unroll
    for (int i = 0; i < 3; ++i) glds16(gsrc + tile * 12288 + i * 1024, lds0 + boff + (wv * 3 + i) * 1024);
  };
  const u16* qrow = Qg + (size_t)(head * SEQ + q) * 32;
  bf16x8 q1 = *(const bf16x8*)(qrow + 8 * h);
  u32x4 q2 = {0u, 0u, 0u, 0u};
  {
    u32x2 t2 = *(const u32x2*)(qrow + 16);
    if (h == 0) { q2[0] = t2[0]; q2[1] = t2[1]; }
  }
  stage(0, 0);
  stage(AT_STAGE, 1);

  const int k1_off = lane * 16, k2_off = 1024 + r * 16;
  const int v_off = 12288 + (h * 24 + (r < 23 ? r : 23)) * 16;

  f32x16 o, s0, s1;
  const f32x16 zero = {0.f, 0.f, 0.f, 0.f, 0.f, 0.f, 0.f, 0.f, 0.f, 0.f, 0.f, 0.f, 0.f, 0.f, 0.f, 0.f};
  o = zero;
  float mref;

  asm volatile("s_waitcnt vmcnt(3)" ::: "memory");
  barrier_raw();
  {
    s0 = MFMA32(*(const bf16x8*)(smem + k1_off), q1, zero);
    s0 = MFMA32(*(const bf16x8*)(smem + k2_off), __builtin_bit_cast(bf16x8, q2), s0);
    s1 = MFMA32(*(const bf16x8*)(smem + 1536 + k1_off), q1, zero);
    s1 = MFMA32(*(const bf16x8*)(smem + 1536 + k2_off), __builtin_bit_cast(bf16x8, q2), s1);
    float mx = fmaxf(s0[0], s1[0]);
#pragma unroll
    for (int i = 1; i < 16; ++i) mx = fmaxf(mx, fmaxf(s0[i], s1[i]));
    mref = rbf(fmaxf(mx, __shfl_xor(mx, 32)) + AT_BIAS);
    if (h == 0) q2[2] = pack_bf2(-mref, 0.f);
#pragma unroll
    for (int i = 0; i < 16; ++i) { s0[i] -= mref; s1[i] -= mref; }
  }
  unsigned cur = 0;
  for (int tile = 0; tile < 8; ++tile) {
    const unsigned nxt = (cur == 2 * AT_STAGE) ? 0 : cur + AT_STAGE;
#pragma unroll
    for (int g = 0; g < 4; ++g) {
      f32x16 n0, n1;
      const char* kb;
      if (g == 3) {
        if (tile < 7) {
          wait_vm0();
          barrier_raw();
          if (tile < 6) stage((nxt == 2 * AT_STAGE) ? 0 : nxt + AT_STAGE, tile + 2);
        }
        kb = smem + nxt;
      } else {
        kb = smem + cur + (g + 1) * 3072;
      }
      n0 = MFMA32(*(const bf16x8*)(kb + k1_off), q1, zero);
      n0 = MFMA32(*(const bf16x8*)(kb + k2_off), __builtin_bit_cast(bf16x8, q2), n0);
      n1 = MFMA32(*(const bf16x8*)(kb + 1536 + k1_off), q1, zero);
      n1 = MFMA32(*(const bf16x8*)(kb + 1536 + k2_off), __builtin_bit_cast(bf16x8, q2), n1);
      unsigned p[16];
#pragma unroll
      for (int i = 0; i < 8; ++i) {
        p[i] = pack_bf2(__builtin_amdgcn_exp2f(s0[2 * i]), __builtin_amdgcn_exp2f(s0[2 * i + 1]));
        p[8 + i] = pack_bf2(__builtin_amdgcn_exp2f(s1[2 * i]), __builtin_amdgcn_exp2f(s1[2 * i + 1]));
      }
      unsigned orr = p[0];
#pragma unroll
      for (int i = 1; i < 16; ++i) orr |= p[i];
      if (__builtin_amdgcn_ballot_w64((orr & 0x40004000u) != 0) != 0) {
        const char* kc = smem + cur + g * 3072;
        float mx = -3.0e38f;
#pragma unroll
        for (int hf = 0; hf < 2; ++hf) {
          f32x16 t = MFMA32(*(const bf16x8*)(kc + hf * 1536 + k1_off), q1, zero);
          t = MFMA32(*(const bf16x8*)(kc + hf * 1536 + k2_off), __builtin_bit_cast(bf16x8, q2), t);
#pragma unroll
          for (int i = 0; i < 16; ++i) mx = fmaxf(mx, t[i]);
          __builtin_amdgcn_sched_barrier(0);
        }
        mx = fmaxf(mx, __shfl_xor(mx, 32));
        const float mnew = rbf(mref + fmaxf(mx + AT_BIAS, 0.f));
        const float delta = mnew - mref;
        const float alpha = __builtin_amdgcn_exp2f(-delta);
        mref = mnew;
        if (h == 0) q2[2] = pack_bf2(-mref, 0.f);
#pragma unroll
        for (int i = 0; i < 16; ++i) { n0[i] -= delta; n1[i] -= delta; o[i] *= alpha; }
#pragma unroll
        for (int hf = 0; hf < 2; ++hf) {
          __builtin_amdgcn_sched_barrier(0);
          f32x16 t = MFMA32(*(const bf16x8*)(kc + hf * 1536 + k1_off), q1, zero);
          t = MFMA32(*(const bf16x8*)(kc + hf * 1536 + k2_off), __builtin_bit_cast(bf16x8, q2), t);
#pragma unroll
          for (int i = 0; i < 8; ++i)
            p[hf * 8 + i] = pack_bf2(__builtin_amdgcn_exp2f(t[2 * i]), __builtin_amdgcn_exp2f(t[2 * i + 1]));
        }
      }
#pragma unroll
      for (int s = 0; s < 4; ++s) {
        u32x4 pv = {p[4 * s], p[4 * s + 1], p[4 * s + 2], p[4 * s + 3]};
        bf16x8 vf = *(const bf16x8*)(smem + cur + (g * 4 + s) * 768 + v_off);
        o = MFMA32(vf, __builtin_bit_cast(bf16x8, pv), o);
      }
      s0 = n0; s1 = n1;
    }
    cur = nxt;
  }
  const float l = __shfl(o[8], r + 32);
  const float inv = 1.f / l;
  u16* orow = obuf + (size_t)q * DM + head * DH + 4 * h;
  {
    u32x2 w0 = {pack_bf2(o[0] * inv, o[1] * inv), pack_bf2(o[2] * inv, o[3] * inv)};
    u32x2 w1 = {pack_bf2(o[4] * inv, o[5] * inv), pack_bf2(o[6] * inv, o[7] * inv)};
    *(u32x2*)(orow) = w0;
    *(u32x2*)(orow + 8) = w1;
    if (h == 0) {
      u32x2 w2 = {pack_bf2(o[8] * inv, o[9] * inv), pack_bf2(o[10] * inv, o[11] * inv)};
      *(u32x2*)(orow + 16) = w2;
    }
  }
}

constexpr int GS_STAGE = 18432;
DI void down_role(char* smem, int b, const u16* __restrict__ hbuf, const u16* __restrict__ wd,
                  const u16* __restrict__ x, float* __restrict__ y) {
  constexpr int NSLOT = 4, NST = DF / 64;
  const int t = (b & 7) * 32 + (b >> 3);
  const int nblk = t & 7, mblk = t >> 3;
  const int tid = threadIdx.x, wave = tid >> 6, lane = tid & 63;
  const int m0 = mblk * 64, n0 = nblk * 80;
  const unsigned lds0 = __builtin_amdgcn_readfirstlane((unsigned)(size_t)smem);
  const unsigned wv = __builtin_amdgcn_readfirstlane(wave);

  unsigned a_vo, b_vo;
  {
    int row = wave * 8 + (lane >> 3);
    a_vo = ((m0 + row) * DF + ((lane & 7) ^ ((row >> 1) & 7)) * 8) * 2;
    b_vo = ((wave * 8 + (((lane >> 1) & 7) ^ ((wave & 1) << 2))) * DM + n0 + (lane >> 4) * 16 + (lane & 1) * 8) * 2;
  }
  auto stage = [&](int st) {
    unsigned sa = lds0 + (st & (NSLOT - 1)) * GS_STAGE;
    glds16s(hbuf + st * 64, a_vo, sa + wv * 1024);
    glds16s(wd + (size_t)st * 64 * DM, b_vo, sa + 8192 + wv * 1280);
    if (lane < 16) glds16s(wd + (size_t)st * 64 * DM + 64, b_vo, sa + 8192 + wv * 1280 + 1024);
  };
#pragma unroll
  for (int st = 0; st < NSLOT - 1; ++st) stage(st);
  const int mh = wave & 1, kk = (wave >> 1) & 1;
  const unsigned par = wv >> 2;
  const int a_off = (mh * 32 + (lane & 15)) * 128 + (((kk * 4 + (lane >> 4)) ^ ((lane >> 1) & 7)) << 4);
  int b_off;
  {
    int g = lane >> 4, q = (lane & 15) >> 2, p = lane & 3;
    b_off = 8192 + (kk * 4 + g) * 1280 + ((g & 1) << 7) + q * 32 + p * 8;
  }
  f32x4 acc[2][5];
#pragma unroll
  for (int i = 0; i < 2; ++i)
#pragma unroll
    for (int j = 0; j < 5; ++j) acc[i][j] = (f32x4){0.f, 0.f, 0.f, 0.f};

  auto step = [&](int st, auto waitc) {
    asm volatile("s_waitcnt vmcnt(%0)" :: "i"(decltype(waitc)::value) : "memory");
    barrier_raw();
    if (st + NSLOT - 1 < NST) stage(st + NSLOT - 1);
    if ((unsigned)(st & 1) == par) {
      const char* sbase = smem + (st & (NSLOT - 1)) * GS_STAGE;
      bf16x8 af0 = *(const bf16x8*)(sbase + a_off);
      bf16x8 af1 = *(const bf16x8*)(sbase + a_off + 2048);
      bf16x8 bfr[5];
#pragma unroll
      for (int j = 0; j < 5; ++j) {
        s16x4 lo = tr_read(sbase + b_off + j * 256);
        s16x4 hi = tr_read(sbase + (b_off ^ 128) + j * 256);
        bfr[j] = __builtin_shufflevector(lo, hi, 0, 1, 2, 3, 4, 5, 6, 7);
      }
#pragma unroll
      for (int j = 0; j < 5; ++j) {
        acc[0][j] = MFMA16(bfr[j], af0, acc[0][j]);
        acc[1][j] = MFMA16(bfr[j], af1, acc[1][j]);
      }
    }
  };
  for (int st = 0; st < NST - (NSLOT - 2); ++st) step(st, IC<3 * (NSLOT - 2)>{});
  static_for<NSLOT - 2>([&](auto i) { step(NST - (NSLOT - 2) + i.value, IC<3 * (NSLOT - 3 - i.value)>{}); });
  barrier_raw();
  if (wv >= 2) {
    float* red = (float*)smem + ((wave - 2) * 64 + lane) * 40;
#pragma unroll
    for (int i = 0; i < 2; ++i)
#pragma unroll
      for (int j = 0; j < 5; ++j) *(f32x4*)(red + (i * 5 + j) * 4) = acc[i][j];
  }
  barrier_raw();
  if (wv < 2) {
#pragma unroll
    for (int i = 0; i < 2; ++i) {
      const size_t gi = (size_t)(m0 + mh * 32 + i * 16 + (lane & 15)) * DM + n0 + (lane >> 4) * 4;
#pragma unroll
      for (int j = 0; j < 5; ++j) {
        f32x4 s = acc[i][j];
#pragma unroll
        for (int w = 0; w < 3; ++w) s += *(const f32x4*)((float*)smem + ((w * 2 + mh) * 64 + lane) * 40 + (i * 5 + j) * 4);
        u32x2 xv = *(const u32x2*)(x + gi + j * 16);
        s[0] += bf_lo(xv[0]); s[1] += bf_hi(xv[0]); s[2] += bf_lo(xv[1]); s[3] += bf_hi(xv[1]);
        *(f32x4*)(y + gi + j * 16) = s;
      }
    }
  }
}

__global__ __launch_bounds__(512, 4) void k2_kernel(const u16* __restrict__ Qg, const u16* __restrict__ Kf,
                                                    const u16* __restrict__ Vf, u16* __restrict__ obuf,
                                                    const u16* __restrict__ hbuf, const u16* __restrict__ wd,
                                                    const u16* __restrict__ x, float* __restrict__ y) {
  __shared__ __attribute__((aligned(16))) char smem[3 * AT_STAGE];
  const int b = blockIdx.x;
  if (b < 256) attn_role(smem, b, Qg, Kf, Vf, obuf);
  else down_role(smem, b - 256, hbuf, wd, x, y);
}

__global__ __launch_bounds__(512, 2) void k3_kernel(const u16* __restrict__ obuf, const u16* __restrict__ wo,
                                                    const float* __restrict__ y, u16* __restrict__ out) {
  __shared__ __attribute__((aligned(16))) char smem[80 * 1280];
  const int b = blockIdx.x;
  const int t = (b & 7) * 32 + (b >> 3);
  const int nblk = t & 7, mblk = t >> 3;
  const int tid = threadIdx.x, wave = tid >> 6, lane = tid & 63;
  const int m0 = mblk * 64, n0 = nblk * 80;
  const unsigned lds0 = __builtin_amdgcn_readfirstlane((unsigned)(size_t)smem);
  const unsigned wv = __builtin_amdgcn_readfirstlane(wave);
  const unsigned b_vo = ((wave * 8 + (((lane >> 1) & 7) ^ ((wave & 1) << 2))) * DM + n0 + (lane >> 4) * 16 + (lane & 1) * 8) * 2;
#pragma unroll
  for (int st = 0; st < 10; ++st) {
    glds16s(wo + st * 64 * DM, b_vo, lds0 + (st * 8 + wv) * 1280);
    if (lane < 16) glds16s(wo + st * 64 * DM + 64, b_vo, lds0 + (st * 8 + wv) * 1280 + 1024);
  }
  const int mh = wave & 1, ks = wave >> 1;
  bf16x8 af[5][2];
  {
    const u16* ap = obuf + (size_t)(m0 + mh * 32 + (lane & 15)) * DM + ks * 32 + (lane >> 4) * 8;
#pragma unroll
    for (int s = 0; s < 5; ++s) {
      af[s][0] = *(const bf16x8*)(ap + s * 128);
      af[s][1] = *(const bf16x8*)(ap + 16 * DM + s * 128);
    }
  }
  f32x4 acc[2][5];
  {
    const float* yp = y + (size_t)(m0 + mh * 32 + (lane & 15)) * DM + n0 + (lane >> 4) * 4;
#pragma unroll
    for (int i = 0; i < 2; ++i)
#pragma unroll
      for (int j = 0; j < 5; ++j) {
        acc[i][j] = (f32x4){0.f, 0.f, 0.f, 0.f};
        if (wv < 2) acc[i][j] = *(const f32x4*)(yp + i * 16 * DM + j * 16);
      }
  }
  int b_off;
  {
    int g = lane >> 4, q = (lane & 15) >> 2, p = lane & 3;
    b_off = (ks * 4 + g) * 1280 + ((g & 1) << 7) + q * 32 + p * 8;
  }
  wait_vm0();
  barrier_raw();
#pragma unroll
  for (int s = 0; s < 5; ++s) {
    bf16x8 bfr[5];
#pragma unroll
    for (int j = 0; j < 5; ++j) {
      s16x4 lo = tr_read(smem + b_off + s * 20480 + j * 256);
      s16x4 hi = tr_read(smem + (b_off ^ 128) + s * 20480 + j * 256);
      bfr[j] = __builtin_shufflevector(lo, hi, 0, 1, 2, 3, 4, 5, 6, 7);
    }
#pragma unroll
    for (int j = 0; j < 5; ++j) {
      acc[0][j] = MFMA16(bfr[j], af[s][0], acc[0][j]);
      acc[1][j] = MFMA16(bfr[j], af[s][1], acc[1][j]);
    }
  }
  barrier_raw();
  if (wv >= 2) {
    float* red = (float*)smem + ((wave - 2) * 64 + lane) * 40;
#pragma unroll
    for (int i = 0; i < 2; ++i)
#pragma unroll
      for (int j = 0; j < 5; ++j) *(f32x4*)(red + (i * 5 + j) * 4) = acc[i][j];
  }
  barrier_raw();
  if (wv < 2) {
#pragma unroll
    for (int i = 0; i < 2; ++i) {
      const size_t gi = (size_t)(m0 + mh * 32 + i * 16 + (lane & 15)) * DM + n0 + (lane >> 4) * 4;
#pragma unroll
      for (int j = 0; j < 5; ++j) {
        f32x4 s = acc[i][j];
#pragma unroll
        for (int w = 0; w < 3; ++w) s += *(const f32x4*)((float*)smem + ((w * 2 + mh) * 64 + lane) * 40 + (i * 5 + j) * 4);
        u32x2 o = {pack_bf2(s[0], s[1]), pack_bf2(s[2], s[3])};
        *(u32x2*)(out + gi + j * 16) = o;
      }
    }
  }
}

extern "C" void kernel_launch(void* const* d_in, const int* in_sizes, int n_in, void* d_out, int out_size,
                              void* d_ws, size_t ws_size, hipStream_t stream) {
  const u16* x = (const u16*)d_in[0];
  const u16* wqkv = (const u16*)d_in[1];
  const u16* wo = (const u16*)d_in[2];
  const u16* wg = (const u16*)d_in[4];
  const u16* wu = (const u16*)d_in[5];
  const u16* wd = (const u16*)d_in[6];
  char* ws = (char*)d_ws;
  u16* xn = (u16*)(ws);
  u16* Qg = (u16*)(ws + (4u << 20));
  u16* Kf = (u16*)(ws + (8u << 20));
  u16* Vf = (u16*)(ws + (12u << 20));
  u16* hb = (u16*)(ws + (16u << 20));
  u16* ob = (u16*)(ws + (24u << 20));
  float* yb = (float*)(ws + (28u << 20));
  ln_kernel<<<SEQ / 4, 256, 0, stream>>>(x, xn);
  gemm1_kernel<<<224, 512, 0, stream>>>(xn, wqkv, wg, wu, Qg, Kf, Vf, hb);
  k2_kernel<<<512, 512, 0, stream>>>(Qg, Kf, Vf, ob, hb, wd, x, yb);
  k3_kernel<<<256, 512, 0, stream>>>(ob, wo, yb, (u16*)d_out);
}
```

```cpp
#include <hip/hip_runtime.h>
#include <stdint.h>

typedef __attribute__((ext_vector_type(8))) short bf16x8;
typedef __attribute__((ext_vector_type(4))) short s16x4;
typedef __attribute__((ext_vector_type(4))) float f32x4;
typedef __attribute__((ext_vector_type(2))) float f32x2;
typedef __attribute__((ext_vector_type(16))) float f32x16;
typedef __attribute__((ext_vector_type(2))) __bf16 bf2_t;
typedef __attribute__((ext_vector_type(4))) unsigned u32x4;
typedef __attribute__((ext_vector_type(2))) unsigned u32x2;
typedef unsigned short u16;
typedef __attribute__((address_space(3))) s16x4 lds_s16x4;

#define DI __device__ __forceinline__

constexpr int SEQ = 2048, DM = 640, NH = 32, DH = 20, DF = 1728;

DI unsigned pack_bf2(float a, float b) {
  f32x2 v = {a, b};
  return __builtin_bit_cast(unsigned, __builtin_convertvector(v, bf2_t));
}
DI float bf_lo(unsigned u) { return __uint_as_float(u << 16); }
DI float bf_hi(unsigned u) { return __uint_as_float(u & 0xffff0000u); }
DI float rbf(float x) { return bf_lo(pack_bf2(x, 0.f)); }

DI void glds16(const void* g, unsigned lds_addr) {
  unsigned keep;
  asm volatile("s_mov_b32 %0, m0\n\ts_mov_b32 m0, %2\n\ts_nop 0\n\tglobal_load_lds_dwordx4 %1, off\n\ts_mov_b32 m0, %0"
               : "=&s"(keep) : "v"(g), "s"(lds_addr) : "memory");
}
DI void glds16s(const void* sbase, unsigned voff, unsigned lds_addr) {
  unsigned keep;
  asm volatile("s_mov_b32 %0, m0\n\ts_mov_b32 m0, %3\n\ts_nop 0\n\tglobal_load_lds_dwordx4 %1, %2\n\ts_mov_b32 m0, %0"
               : "=&s"(keep) : "v"(voff), "s"(sbase), "s"(lds_addr) : "memory");
}
DI void wait_vm0() { asm volatile("s_waitcnt vmcnt(0)" ::: "memory"); }
DI void barrier_raw() {
  asm volatile("s_waitcnt lgkmcnt(0)" ::: "memory");
  __builtin_amdgcn_s_barrier();
  asm volatile("" ::: "memory");
}
template <int V> struct IC { static constexpr int value = V; };
template <int N, int I = 0, class F> DI void static_for(F&& f) {
  if constexpr (I < N) { f(IC<I>{}); static_for<N, I + 1>(f); }
}
DI s16x4 tr_read(const char* lds) {
  return __builtin_amdgcn_ds_read_tr16_b64_v4i16((lds_s16x4*)lds);
}

__global__ __launch_bounds__(256) void ln_kernel(const u16* __restrict__ x, u16* __restrict__ xn) {
  const int row = blockIdx.x * 4 + (threadIdx.x >> 6), lane = threadIdx.x & 63;
  const unsigned* xr = (const unsigned*)(x + row * DM);
  float f[10];
#pragma unroll
  for (int j = 0; j < 5; ++j) {
    unsigned v = xr[lane + 64 * j];
    f[2 * j] = bf_lo(v);
    f[2 * j + 1] = bf_hi(v);
  }
  float s = 0.f;
#pragma unroll
  for (int j = 0; j < 10; ++j) s += f[j];
#pragma unroll
  for (int o = 32; o > 0; o >>= 1) s += __shfl_xor(s, o);
  const float mean = s * (1.f / DM);
  float q = 0.f;
#pragma unroll
  for (int j = 0; j < 10; ++j) { float d = f[j] - mean; q += d * d; }
#pragma unroll
  for (int o = 32; o > 0; o >>= 1) q += __shfl_xor(q, o);
  const float mb = rbf(mean);
  const float vb = rbf(q * (1.f / DM));
  const float den = rbf(sqrtf(rbf(vb + 1e-5f)));
  unsigned* o = (unsigned*)(xn + row * DM);
#pragma unroll
  for (int j = 0; j < 5; ++j)
    o[lane + 64 * j] = pack_bf2(rbf(f[2 * j] - mb) / den, rbf(f[2 * j + 1] - mb) / den);
}

constexpr int G1_BOFF = 3 * 32768;
__global__ __launch_bounds__(512, 2) void gemm1_kernel(const u16* __restrict__ xn, const u16* __restrict__ wqkv,
                                                       const u16* __restrict__ wg, const u16* __restrict__ wu,
                                                       u16* __restrict__ Qg, u16* __restrict__ Kf,
                                                       u16* __restrict__ Vf, u16* __restrict__ hbuf,
                                                       const u16* __restrict__ wd, const u16* __restrict__ wo,
                                                       u16* __restrict__ Bt) {
  __shared__ __attribute__((aligned(16))) char smem[G1_BOFF + 2 * 24576];
  const int b = blockIdx.x;
  if (b >= 224) {
    for (int it = (b - 224) * 512 + threadIdx.x; it < 2368 * 80; it += 32 * 512) {
      const int k = it / 80, c8 = it - k * 80;
      const u16* src = (k < DF) ? (wd + (size_t)k * DM + c8 * 8) : (wo + (size_t)(k - DF) * DM + c8 * 8);
      const int nb = c8 / 10, r10 = c8 - nb * 10, kb = k >> 3;
      u16* dst = Bt + ((size_t)(nb * 296 + kb) * 640 + (r10 >> 1) * 128 + ((k & 7) ^ ((kb & 1) << 2)) * 16 + (r10 & 1) * 8);
      *(u32x4*)dst = *(const u32x4*)src;
    }
    return;
  }
  const int t = (b & 7) * 28 + (b >> 3);
  const int nblk = t >> 3, mblk = t & 7;
  const int tid = threadIdx.x, wave = tid >> 6, lane = tid & 63;
  const int wm = wave >> 1, wn = wave & 1;
  const int m0 = mblk * 256;
  const bool isqkv = nblk < 10;

  const u16* a_src[4];
#pragma unroll
  for (int i = 0; i < 4; ++i) {
    int row = wave * 32 + i * 8 + (lane >> 3);
    int c = (lane & 7) ^ ((row >> 1) & 7);
    a_src[i] = xn + (size_t)(m0 + row) * DM + c * 8;
  }
  const int ldb = isqkv ? 3 * DM : DF;
  const u16* b_src[3];
#pragma unroll
  for (int i = 0; i < 3; ++i) {
    int kb = wave;
    int nt = i * 4 + (lane >> 4);
    int rr = ((lane >> 1) & 7) ^ ((kb & 1) << 2);
    int k = kb * 8 + rr;
    const u16* base;
    if (isqkv) base = wqkv + nblk * 192 + nt * 16;
    else base = (nt < 6) ? (wg + (nblk - 10) * 96 + nt * 16) : (wu + (nblk - 10) * 96 + (nt - 6) * 16);
    b_src[i] = base + (size_t)k * ldb + (lane & 1) * 8;
  }
  const unsigned lds0 = __builtin_amdgcn_readfirstlane((unsigned)(size_t)smem);
  const unsigned wv = __builtin_amdgcn_readfirstlane(wave);
  auto stage_a = [&](int kt, int g) { glds16(a_src[g] + kt * 64, lds0 + (kt % 3) * 32768 + (wv * 4 + g) * 1024); };
  auto stage_b = [&](int kt, int g) {
    glds16(b_src[g] + (size_t)kt * 64 * ldb, lds0 + G1_BOFF + (kt & 1) * 24576 + (wv * 3 + g) * 1024);
  };
#pragma unroll
  for (int kt = 0; kt < 2; ++kt) {
#pragma unroll
    for (int g = 0; g < 4; ++g) stage_a(kt, g);
#pragma unroll
    for (int g = 0; g < 3; ++g) stage_b(kt, g);
  }

  const int a_off = (wm * 64 + (lane & 15)) * 128 + (((lane >> 4) ^ ((lane >> 1) & 7)) << 4);
  int b_off[2];
  {
    int g = lane >> 4, q = (lane & 15) >> 2, p = lane & 3;
    int base = G1_BOFF + g * 3072 + ((g & 1) << 7) + q * 32 + p * 8;
    b_off[0] = base + (isqkv ? wn * 6 : wn * 3) * 256;
    b_off[1] = base + (isqkv ? wn * 6 + 3 : 6 + wn * 3) * 256;
  }

  f32x4 acc[4][6];
#pragma unroll
  for (int i = 0; i < 4; ++i)
#pragma unroll
    for (int j = 0; j < 6; ++j) acc[i][j] = (f32x4){0.f, 0.f, 0.f, 0.f};

  bf16x8 af0[4], bf0[6], af1[4], bf1[6];
  auto read_q = [&](bf16x8 (&af)[4], bf16x8 (&bfr)[6], int kt, int kk, int g) {
    const char* sa = smem + (kt % 3) * 32768;
    const char* sb = smem + (kt & 1) * 24576;
    auto rb = [&](int j) {
      s16x4 lo = tr_read(sb + b_off[j / 3] + (j % 3) * 256 + kk * 12288);
      s16x4 hi = tr_read(sb + (b_off[j / 3] ^ 128) + (j % 3) * 256 + kk * 12288);
      bfr[j] = __builtin_shufflevector(lo, hi, 0, 1, 2, 3, 4, 5, 6, 7);
    };
    auto ra = [&](int i) { af[i] = *(const bf16x8*)(sa + (a_off ^ (kk << 6)) + i * 2048); };
    if (g == 0) { ra(0); rb(0); rb(1); }
    if (g == 1) { rb(2); rb(3); }
    if (g == 2) { rb(4); rb(5); }
    if (g == 3) { ra(1); ra(2); ra(3); }
  };
  auto mma_q = [&](bf16x8 (&af)[4], bf16x8 (&bfr)[6], int i) {
#pragma unroll
    for (int j = 0; j < 6; ++j)
      acc[i][j] = __builtin_amdgcn_mfma_f32_16x16x32_bf16(bfr[j], af[i], acc[i][j], 0, 0, 0);
  };

  asm volatile("s_waitcnt vmcnt(7)" ::: "memory");
  barrier_raw();
#pragma unroll
  for (int g = 0; g < 4; ++g) read_q(af0, bf0, 0, 0, g);
#pragma unroll
  for (int kt = 0; kt < 10; ++kt) {
#pragma unroll
    for (int g = 0; g < 4; ++g) {
      __builtin_amdgcn_sched_barrier(0);
      read_q(af1, bf1, kt, 1, g);
      mma_q(af0, bf0, g);
      __builtin_amdgcn_sched_barrier(0);
      if (kt + 2 < 10) stage_a(kt + 2, g);
    }
    __builtin_amdgcn_sched_barrier(0);
    if (kt + 1 < 10) {
      if (kt + 2 < 10) asm volatile("s_waitcnt vmcnt(4)" ::: "memory");
      else wait_vm0();
      barrier_raw();
    }
#pragma unroll
    for (int g = 0; g < 4; ++g) {
      __builtin_amdgcn_sched_barrier(0);
      if (kt + 1 < 10) read_q(af0, bf0, kt + 1, 0, g);
      mma_q(af1, bf1, g);
      __builtin_amdgcn_sched_barrier(0);
      if (kt + 2 < 10 && g < 3) stage_b(kt + 2, g);
    }
  }
  __builtin_amdgcn_sched_barrier(0);
  barrier_raw();

  int tz = threadIdx.x;
  asm volatile("" : "+v"(tz));
  const int e_wave = tz >> 6, e_lane = tz & 63, e_wm = e_wave >> 1, e_wn = e_wave & 1;
  const int ln4 = (e_lane >> 4) * 4;
  if (isqkv) {
    const float qscale = 0.22360679774997896f * 1.4426950408889634f;
    char* wtr = smem + e_wave * 512;
#pragma unroll
    for (int j = 0; j < 6; ++j) {
      const int nb = nblk * 192 + (e_wn * 6 + j) * 16;
      const int sec = nb / DM;
      if (sec < 2) {
        const int rr = nb + ln4 - sec * DM, head = rr / DH, d = rr - head * DH, d4 = d >> 2;
#pragma unroll
        for (int i = 0; i < 4; ++i) {
          const int tok = m0 + e_wm * 64 + i * 16 + (e_lane & 15);
          f32x4 v = acc[i][j];
          if (sec == 0) {
            u32x2 o = {pack_bf2(v[0] * qscale, v[1] * qscale), pack_bf2(v[2] * qscale, v[3] * qscale)};
            *(u32x2*)(Qg + ((size_t)(head * SEQ + tok) * 32 + d)) = o;
          } else {
            char* kb = (char*)Kf + (size_t)(head * 64 + (tok >> 5)) * 1536;
            const int r = tok & 31;
            unsigned p0 = pack_bf2(v[0], v[1]), p1 = pack_bf2(v[2], v[3]);
            if (d4 < 4) {
              u32x2 o = {p0, p1};
              *(u32x2*)(kb + ((d4 >> 1) * 32 + r) * 16 + (d4 & 1) * 8) = o;
            } else {
              u32x4 o = {p0, p1, 0x00003F80u, 0u};
              *(u32x4*)(kb + 1024 + r * 16) = o;
            }
          }
        }
      } else {
        const int rr = nb + (e_lane & 15) - 2 * DM, head = rr / DH, d = rr - head * DH;
        const int g = e_lane >> 4;
#pragma unroll
        for (int i = 0; i < 4; ++i) {
          f32x4 v = acc[i][j];
          u32x2 w = {pack_bf2(v[0], v[1]), pack_bf2(v[2], v[3])};
          *(u32x2*)(wtr + (e_lane & 15) * 32 + g * 8) = w;
          s16x4 tv = tr_read(wtr + g * 128 + ((e_lane & 15) >> 2) * 32 + (e_lane & 3) * 8);
          const int s = (m0 + e_wm * 64 + i * 16) >> 4;
          u16* vb = Vf + ((size_t)(head * 128 + s) * 48 + (g & 1) * 24 + d) * 8 + (g >> 1) * 4;
          *(s16x4*)vb = tv;
          if (d == DH - 1) { s16x4 one = {0x3F80, 0x3F80, 0x3F80, 0x3F80}; *(s16x4*)(vb + 8) = one; }
        }
      }
    }
  } else {
#pragma unroll
    for (int j = 0; j < 3; ++j) {
      const int f = (nblk - 10) * 96 + (e_wn * 3 + j) * 16 + ln4;
#pragma unroll
      for (int i = 0; i < 4; ++i) {
        const int tok = m0 + e_wm * 64 + i * 16 + (e_lane & 15);
        float hv[4];
#pragma unroll
        for (int r = 0; r < 4; ++r) {
          const float g = acc[i][j][r], u = acc[i][j + 3][r];
          const float e = __builtin_amdgcn_exp2f(g * __builtin_fmaf(g * g, -0.10294324f, -2.30220819f));
          hv[r] = (g * u) * __builtin_amdgcn_rcpf(1.f + e);
        }
        u32x2 o = {pack_bf2(hv[0], hv[1]), pack_bf2(hv[2], hv[3])};
        *(u32x2*)(hbuf + (size_t)tok * DF + f) = o;
      }
    }
  }
}

constexpr int AT_STAGE = 24576;
constexpr float AT_BIAS = 8.f;
#define MFMA32(a, b, c) __builtin_amdgcn_mfma_f32_32x32x16_bf16((a), (b), (c), 0, 0, 0)
#define MFMA16(a, b, c) __builtin_amdgcn_mfma_f32_16x16x32_bf16((a), (b), (c), 0, 0, 0)

DI void attn_role(char* smem, int b, const u16* __restrict__ Qg, const u16* __restrict__ Kf,
                  const u16* __restrict__ Vf, u16* __restrict__ obuf) {
  const int head = (b & 7) * 4 + ((b >> 3) & 3), qb = b >> 5;
  const int tid = threadIdx.x, wave = tid >> 6, lane = tid & 63;
  const int r = lane & 31, h = lane >> 5;
  const int q = qb * 256 + wave * 32 + r;

  const unsigned lds0 = __builtin_amdgcn_readfirstlane((unsigned)(size_t)smem);
  const unsigned wv = __builtin_amdgcn_readfirstlane(wave);
  const char* gsrc = ((wv < 4) ? (const char*)Kf + wv * 3072 : (const char*)Vf + (wv - 4) * 3072) +
                     (size_t)head * 98304 + lane * 16;
  auto stage = [&](unsigned boff, int tile) {
#pragma unroll
    for (int i = 0; i < 3; ++i) glds16(gsrc + tile * 12288 + i * 1024, lds0 + boff + (wv * 3 + i) * 1024);
  };
  const u16* qrow = Qg + (size_t)(head * SEQ + q) * 32;
  bf16x8 q1 = *(const bf16x8*)(qrow + 8 * h);
  u32x4 q2 = {0u, 0u, 0u, 0u};
  {
    u32x2 t2 = *(const u32x2*)(qrow + 16);
    if (h == 0) { q2[0] = t2[0]; q2[1] = t2[1]; }
  }
  stage(0, 0);
  stage(AT_STAGE, 1);

  const int k1_off = lane * 16, k2_off = 1024 + r * 16;
  const int v_off = 12288 + (h * 24 + (r < 23 ? r : 23)) * 16;

  f32x16 o, s0, s1;
  const f32x16 zero = {0.f, 0.f, 0.f, 0.f, 0.f, 0.f, 0.f, 0.f, 0.f, 0.f, 0.f, 0.f, 0.f, 0.f, 0.f, 0.f};
  o = zero;
  float mref;

  asm volatile("s_waitcnt vmcnt(3)" ::: "memory");
  barrier_raw();
  {
    s0 = MFMA32(*(const bf16x8*)(smem + k1_off), q1, zero);
    s0 = MFMA32(*(const bf16x8*)(smem + k2_off), __builtin_bit_cast(bf16x8, q2), s0);
    s1 = MFMA32(*(const bf16x8*)(smem + 1536 + k1_off), q1, zero);
    s1 = MFMA32(*(const bf16x8*)(smem + 1536 + k2_off), __builtin_bit_cast(bf16x8, q2), s1);
    float mx = fmaxf(s0[0], s1[0]);
#pragma unroll
    for (int i = 1; i < 16; ++i) mx = fmaxf(mx, fmaxf(s0[i], s1[i]));
    mref = rbf(fmaxf(mx, __shfl_xor(mx, 32)) + AT_BIAS);
    if (h == 0) q2[2] = pack_bf2(-mref, 0.f);
#pragma unroll
    for (int i = 0; i < 16; ++i) { s0[i] -= mref; s1[i] -= mref; }
  }
  unsigned cur = 0;
  for (int tile = 0; tile < 8; ++tile) {
    const unsigned nxt = (cur == 2 * AT_STAGE) ? 0 : cur + AT_STAGE;
#pragma unroll
    for (int g = 0; g < 4; ++g) {
      f32x16 n0, n1;
      const char* kb;
      if (g == 3) {
        if (tile < 7) {
          wait_vm0();
          barrier_raw();
          if (tile < 6) stage((nxt == 2 * AT_STAGE) ? 0 : nxt + AT_STAGE, tile + 2);
        }
        kb = smem + nxt;
      } else {
        kb = smem + cur + (g + 1) * 3072;
      }
      n0 = MFMA32(*(const bf16x8*)(kb + k1_off), q1, zero);
      n0 = MFMA32(*(const bf16x8*)(kb + k2_off), __builtin_bit_cast(bf16x8, q2), n0);
      n1 = MFMA32(*(const bf16x8*)(kb + 1536 + k1_off), q1, zero);
      n1 = MFMA32(*(const bf16x8*)(kb + 1536 + k2_off), __builtin_bit_cast(bf16x8, q2), n1);
      unsigned p[16];
#pragma unroll
      for (int i = 0; i < 8; ++i) {
        p[i] = pack_bf2(__builtin_amdgcn_exp2f(s0[2 * i]), __builtin_amdgcn_exp2f(s0[2 * i + 1]));
        p[8 + i] = pack_bf2(__builtin_amdgcn_exp2f(s1[2 * i]), __builtin_amdgcn_exp2f(s1[2 * i + 1]));
      }
      unsigned orr = p[0];
#pragma unroll
      for (int i = 1; i < 16; ++i) orr |= p[i];
      if (__builtin_amdgcn_ballot_w64((orr & 0x40004000u) != 0) != 0) {
        const char* kc = smem + cur + g * 3072;
        float mx = -3.0e38f;
#pragma unroll
        for (int hf = 0; hf < 2; ++hf) {
          f32x16 t = MFMA32(*(const bf16x8*)(kc + hf * 1536 + k1_off), q1, zero);
          t = MFMA32(*(const bf16x8*)(kc + hf * 1536 + k2_off), __builtin_bit_cast(bf16x8, q2), t);
#pragma unroll
          for (int i = 0; i < 16; ++i) mx = fmaxf(mx, t[i]);
          __builtin_amdgcn_sched_barrier(0);
        }
        mx = fmaxf(mx, __shfl_xor(mx, 32));
        const float mnew = rbf(mref + fmaxf(mx + AT_BIAS, 0.f));
        const float delta = mnew - mref;
        const float alpha = __builtin_amdgcn_exp2f(-delta);
        mref = mnew;
        if (h == 0) q2[2] = pack_bf2(-mref, 0.f);
#pragma unroll
        for (int i = 0; i < 16; ++i) { n0[i] -= delta; n1[i] -= delta; o[i] *= alpha; }
#pragma unroll
        for (int hf = 0; hf < 2; ++hf) {
          __builtin_amdgcn_sched_barrier(0);
          f32x16 t = MFMA32(*(const bf16x8*)(kc + hf * 1536 + k1_off), q1, zero);
          t = MFMA32(*(const bf16x8*)(kc + hf * 1536 + k2_off), __builtin_bit_cast(bf16x8, q2), t);
#pragma unroll
          for (int i = 0; i < 8; ++i)
            p[hf * 8 + i] = pack_bf2(__builtin_amdgcn_exp2f(t[2 * i]), __builtin_amdgcn_exp2f(t[2 * i + 1]));
        }
      }
#pragma unroll
      for (int s = 0; s < 4; ++s) {
        u32x4 pv = {p[4 * s], p[4 * s + 1], p[4 * s + 2], p[4 * s + 3]};
        bf16x8 vf = *(const bf16x8*)(smem + cur + (g * 4 + s) * 768 + v_off);
        o = MFMA32(vf, __builtin_bit_cast(bf16x8, pv), o);
      }
      s0 = n0; s1 = n1;
    }
    cur = nxt;
  }
  const float l = __shfl(o[8], r + 32);
  const float inv = 1.f / l;
  auto ostore = [&](int d, u32x2 w) {
    const int col = head * DH + d;
    *(u32x2*)(obuf + ((size_t)((q >> 4) * 20 + (col >> 5)) * 64 + ((col >> 3) & 3) * 16 + (q & 15)) * 8 + (col & 7)) = w;
  };
  {
    u32x2 w0 = {pack_bf2(o[0] * inv, o[1] * inv), pack_bf2(o[2] * inv, o[3] * inv)};
    u32x2 w1 = {pack_bf2(o[4] * inv, o[5] * inv), pack_bf2(o[6] * inv, o[7] * inv)};
    ostore(4 * h, w0);
    ostore(4 * h + 8, w1);
    if (h == 0) {
      u32x2 w2 = {pack_bf2(o[8] * inv, o[9] * inv), pack_bf2(o[10] * inv, o[11] * inv)};
      ostore(16, w2);
    }
  }
}

constexpr int GS_STAGE = 18432;
DI void down_role(char* smem, int b, const u16* __restrict__ hbuf, const u16* __restrict__ Bt,
                  const u16* __restrict__ x, float* __restrict__ y) {
  constexpr int NSLOT = 4, NST = DF / 64;
  const int t = (b & 7) * 32 + (b >> 3);
  const int nblk = t & 7, mblk = t >> 3;
  const int tid = threadIdx.x, wave = tid >> 6, lane = tid & 63;
  const int m0 = mblk * 64, n0 = nblk * 80;
  const unsigned lds0 = __builtin_amdgcn_readfirstlane((unsigned)(size_t)smem);
  const unsigned wv = __builtin_amdgcn_readfirstlane(wave);

  unsigned a_vo, b_vo;
  {
    int row = wave * 8 + (lane >> 3);
    a_vo = ((m0 + row) * DF + ((lane & 7) ^ ((row >> 1) & 7)) * 8) * 2;
    b_vo = (nblk * 296 + wave) * 1280 + lane * 16;
  }
  auto stage = [&](int st) {
    unsigned sa = lds0 + (st & (NSLOT - 1)) * GS_STAGE;
    glds16s(hbuf + st * 64, a_vo, sa + wv * 1024);
    glds16s(Bt + st * 5120, b_vo, sa + 8192 + wv * 1280);
    if (lane < 16) glds16s(Bt + st * 5120 + 512, b_vo, sa + 8192 + wv * 1280 + 1024);
  };
#pragma unroll
  for (int st = 0; st < NSLOT - 1; ++st) stage(st);
  const int mh = wave & 1, kk = (wave >> 1) & 1;
  const unsigned par = wv >> 2;
  const int a_off = (mh * 32 + (lane & 15)) * 128 + (((kk * 4 + (lane >> 4)) ^ ((lane >> 1) & 7)) << 4);
  int b_off;
  {
    int g = lane >> 4, q = (lane & 15) >> 2, p = lane & 3;
    b_off = 8192 + (kk * 4 + g) * 1280 + ((g & 1) << 7) + q * 32 + p * 8;
  }
  f32x4 acc[2][5];
#pragma unroll
  for (int i = 0; i < 2; ++i)
#pragma unroll
    for (int j = 0; j < 5; ++j) acc[i][j] = (f32x4){0.f, 0.f, 0.f, 0.f};

  auto step = [&](int st, auto waitc) {
    asm volatile("s_waitcnt vmcnt(%0)" :: "i"(decltype(waitc)::value) : "memory");
    barrier_raw();
    if (st + NSLOT - 1 < NST) stage(st + NSLOT - 1);
    if ((unsigned)(st & 1) == par) {
      const char* sbase = smem + (st & (NSLOT - 1)) * GS_STAGE;
      bf16x8 af0 = *(const bf16x8*)(sbase + a_off);
      bf16x8 af1 = *(const bf16x8*)(sbase + a_off + 2048);
      bf16x8 bfr[5];
#pragma unroll
      for (int j = 0; j < 5; ++j) {
        s16x4 lo = tr_read(sbase + b_off + j * 256);
        s16x4 hi = tr_read(sbase + (b_off ^ 128) + j * 256);
        bfr[j] = __builtin_shufflevector(lo, hi, 0, 1, 2, 3, 4, 5, 6, 7);
      }
#pragma unroll
      for (int j = 0; j < 5; ++j) {
        acc[0][j] = MFMA16(bfr[j], af0, acc[0][j]);
        acc[1][j] = MFMA16(bfr[j], af1, acc[1][j]);
      }
    }
  };
  for (int st = 0; st < NST - (NSLOT - 2); ++st) step(st, IC<3 * (NSLOT - 2)>{});
  static_for<NSLOT - 2>([&](auto i) { step(NST - (NSLOT - 2) + i.value, IC<3 * (NSLOT - 3 - i.value)>{}); });
  barrier_raw();
  if (wv >= 2) {
    float* red = (float*)smem + ((wave - 2) * 64 + lane) * 40;
#pragma unroll
    for (int i = 0; i < 2; ++i)
#pragma unroll
      for (int j = 0; j < 5; ++j) *(f32x4*)(red + (i * 5 + j) * 4) = acc[i][j];
  }
  barrier_raw();
  if (wv < 2) {
#pragma unroll
    for (int i = 0; i < 2; ++i) {
      const size_t gi = (size_t)(m0 + mh * 32 + i * 16 + (lane & 15)) * DM + n0 + (lane >> 4) * 4;
#pragma unroll
      for (int j = 0; j < 5; ++j) {
        f32x4 s = acc[i][j];
#pragma unroll
        for (int w = 0; w < 3; ++w) s += *(const f32x4*)((float*)smem + ((w * 2 + mh) * 64 + lane) * 40 + (i * 5 + j) * 4);
        u32x2 xv = *(const u32x2*)(x + gi + j * 16);
        s[0] += bf_lo(xv[0]); s[1] += bf_hi(xv[0]); s[2] += bf_lo(xv[1]); s[3] += bf_hi(xv[1]);
        *(f32x4*)(y + ((size_t)(t * 20 + mh * 10 + i * 5 + j) * 64 + lane) * 4) = s;
      }
    }
  }
}

__global__ __launch_bounds__(512, 4) void k2_kernel(const u16* __restrict__ Qg, const u16* __restrict__ Kf,
                                                    const u16* __restrict__ Vf, u16* __restrict__ obuf,
                                                    const u16* __restrict__ hbuf, const u16* __restrict__ Bt,
                                                    const u16* __restrict__ x, float* __restrict__ y) {
  __shared__ __attribute__((aligned(16))) char smem[3 * AT_STAGE];
  const int b = blockIdx.x;
  if (b < 256) attn_role(smem, b, Qg, Kf, Vf, obuf);
  else down_role(smem, b - 256, hbuf, Bt, x, y);
}

__global__ __launch_bounds__(512, 2) void k3_kernel(const u16* __restrict__ obuf, const u16* __restrict__ Bt,
                                                    const float* __restrict__ y, u16* __restrict__ out) {
  __shared__ __attribute__((aligned(16))) char smem[80 * 1280];
  const int b = blockIdx.x;
  const int t = (b & 7) * 32 + (b >> 3);
  const int nblk = t & 7, mblk = t >> 3;
  const int tid = threadIdx.x, wave = tid >> 6, lane = tid & 63;
  const int m0 = mblk * 64, n0 = nblk * 80;
  const unsigned lds0 = __builtin_amdgcn_readfirstlane((unsigned)(size_t)smem);
  const unsigned wv = __builtin_amdgcn_readfirstlane(wave);
  {
    const unsigned b_vo = (nblk * 296 + DF / 8) * 1280 + wave * 1024 + lane * 16;
#pragma unroll
    for (int i = 0; i < 13; ++i)
      if (i < 12 || wv < 4) glds16s(Bt + i * 4096, b_vo, lds0 + (i * 8 + wv) * 1024);
  }
  const int mh = wave & 1, ks = wave >> 1;
  bf16x8 af[5][2];
  {
    const u16* ap = obuf + ((size_t)((mblk * 4 + mh * 2) * 20 + ks) * 64 + lane) * 8;
#pragma unroll
    for (int s = 0; s < 5; ++s) {
      af[s][0] = *(const bf16x8*)(ap + s * 4 * 512);
      af[s][1] = *(const bf16x8*)(ap + 20 * 512 + s * 4 * 512);
    }
  }
  f32x4 yv[3];
  {
    const float* yp = y + ((size_t)(t * 20 + wave) * 64 + lane) * 4;
    yv[0] = *(const f32x4*)(yp);
    yv[1] = *(const f32x4*)(yp + 8 * 256);
    yv[2] = *(const f32x4*)(yp + (wv < 4 ? 16 : 8) * 256);
  }
  f32x4 acc[2][5];
#pragma unroll
  for (int i = 0; i < 2; ++i)
#pragma unroll
    for (int j = 0; j < 5; ++j) acc[i][j] = (f32x4){0.f, 0.f, 0.f, 0.f};
  int b_off;
  {
    int g = lane >> 4, q = (lane & 15) >> 2, p = lane & 3;
    b_off = (ks * 4 + g) * 1280 + ((g & 1) << 7) + q * 32 + p * 8;
  }
  wait_vm0();
  barrier_raw();
#pragma unroll
  for (int s = 0; s < 5; ++s) {
    bf16x8 bfr[5];
#pragma unroll
    for (int j = 0; j < 5; ++j) {
      s16x4 lo = tr_read(smem + b_off + s * 20480 + j * 256);
      s16x4 hi = tr_read(smem + (b_off ^ 128) + s * 20480 + j * 256);
      bfr[j] = __builtin_shufflevector(lo, hi, 0, 1, 2, 3, 4, 5, 6, 7);
    }
#pragma unroll
    for (int j = 0; j < 5; ++j) {
      acc[0][j] = MFMA16(bfr[j], af[s][0], acc[0][j]);
      acc[1][j] = MFMA16(bfr[j], af[s][1], acc[1][j]);
    }
  }
  barrier_raw();
  {
    f32x4* red = (f32x4*)smem + (ks * 20 + mh * 10) * 64 + lane;
#pragma unroll
    for (int i = 0; i < 2; ++i)
#pragma unroll
      for (int j = 0; j < 5; ++j) red[(i * 5 + j) * 64] = acc[i][j];
  }
  barrier_raw();
#pragma unroll
  for (int u = 0; u < 3; ++u) {
    const int tile = wave + u * 8;
    if (u < 2 || wv < 4) {
      f32x4 s = yv[u];
#pragma unroll
      for (int w = 0; w < 4; ++w) s += ((const f32x4*)smem)[(w * 20 + tile) * 64 + lane];
      const int rt = tile / 5, j = tile - rt * 5;
      u32x2 o = {pack_bf2(s[0], s[1]), pack_bf2(s[2], s[3])};
      *(u32x2*)(out + (size_t)(m0 + rt * 16 + (lane & 15)) * DM + n0 + j * 16 + (lane >> 4) * 4) = o;
    }
  }
}

extern "C" void kernel_launch(void* const* d_in, const int* in_sizes, int n_in, void* d_out, int out_size,
                              void* d_ws, size_t ws_size, hipStream_t stream) {
  const u16* x = (const u16*)d_in[0];
  const u16* wqkv = (const u16*)d_in[1];
  const u16* wo = (const u16*)d_in[2];
  const u16* wg = (const u16*)d_in[4];
  const u16* wu = (const u16*)d_in[5];
  const u16* wd = (const u16*)d_in[6];
  char* ws = (char*)d_ws;
  u16* xn = (u16*)(ws);
  u16* Qg = (u16*)(ws + (4u << 20));
  u16* Kf = (u16*)(ws + (8u << 20));
  u16* Vf = (u16*)(ws + (12u << 20));
  u16* hb = (u16*)(ws + (16u << 20));
  u16* ob = (u16*)(ws + (24u << 20));
  float* yb = (float*)(ws + (28u << 20));
  u16* Bt = (u16*)(ws + (34u << 20));
  ln_kernel<<<SEQ / 4, 256, 0, stream>>>(x, xn);
  gemm1_kernel<<<256, 512, 0, stream>>>(xn, wqkv, wg, wu, Qg, Kf, Vf, hb, wd, wo, Bt);
  k2_kernel<<<512, 512, 0, stream>>>(Qg, Kf, Vf, ob, hb, Bt, x, yb);
  k3_kernel<<<256, 512, 0, stream>>>(ob, Bt, yb, (u16*)d_out);
}
```

```cpp
#include <hip/hip_runtime.h>
#include <stdint.h>

typedef __attribute__((ext_vector_type(8))) short bf16x8;
typedef __attribute__((ext_vector_type(4))) short s16x4;
typedef __attribute__((ext_vector_type(4))) float f32x4;
typedef __attribute__((ext_vector_type(2))) float f32x2;
typedef __attribute__((ext_vector_type(16))) float f32x16;
typedef __attribute__((ext_vector_type(2))) __bf16 bf2_t;
typedef __attribute__((ext_vector_type(8))) __bf16 bf8_t;
typedef __attribute__((ext_vector_type(4))) unsigned u32x4;
typedef __attribute__((ext_vector_type(2))) unsigned u32x2;
typedef unsigned short u16;
typedef __attribute__((address_space(3))) s16x4 lds_s16x4;

#define DI __device__ __forceinline__

constexpr int SEQ = 2048, DM = 640, NH = 32, DH = 20, DF = 1728;

DI unsigned pack_bf2(float a, float b) {
  f32x2 v = {a, b};
  return __builtin_bit_cast(unsigned, __builtin_convertvector(v, bf2_t));
}
DI float bf_lo(unsigned u) { return __uint_as_float(u << 16); }
DI float bf_hi(unsigned u) { return __uint_as_float(u & 0xffff0000u); }
DI float rbf(float x) { return bf_lo(pack_bf2(x, 0.f)); }

DI void glds16(const void* g, unsigned lds_addr) {
  unsigned keep;
  asm volatile("s_mov_b32 %0, m0\n\ts_mov_b32 m0, %2\n\ts_nop 0\n\tglobal_load_lds_dwordx4 %1, off\n\ts_mov_b32 m0, %0"
               : "=&s"(keep) : "v"(g), "s"(lds_addr) : "memory");
}
DI void glds16s(const void* sbase, unsigned voff, unsigned lds_addr) {
  unsigned keep;
  asm volatile("s_mov_b32 %0, m0\n\ts_mov_b32 m0, %3\n\ts_nop 0\n\tglobal_load_lds_dwordx4 %1, %2\n\ts_mov_b32 m0, %0"
               : "=&s"(keep) : "v"(voff), "s"(sbase), "s"(lds_addr) : "memory");
}
DI void wait_vm0() { asm volatile("s_waitcnt vmcnt(0)" ::: "memory"); }
DI void barrier_raw() {
  asm volatile("s_waitcnt lgkmcnt(0)" ::: "memory");
  __builtin_amdgcn_s_barrier();
  asm volatile("" ::: "memory");
}
template <int V> struct IC { static constexpr int value = V; };
template <int N, int I = 0, class F> DI void static_for(F&& f) {
  if constexpr (I < N) { f(IC<I>{}); static_for<N, I + 1>(f); }
}
DI s16x4 tr_read(const char* lds) {
  return __builtin_amdgcn_ds_read_tr16_b64_v4i16((lds_s16x4*)lds);
}

constexpr int G1_BOFF = 3 * 32768;
__global__ __launch_bounds__(512, 2) void gemm1_kernel(const u16* __restrict__ x, const u16* __restrict__ wqkv,
                                                       const u16* __restrict__ wg, const u16* __restrict__ wu,
                                                       u16* __restrict__ Qg, u16* __restrict__ Kf,
                                                       u16* __restrict__ Vf, u16* __restrict__ hbuf,
                                                       const u16* __restrict__ wd, const u16* __restrict__ wo,
                                                       u16* __restrict__ Bt) {
  __shared__ __attribute__((aligned(16))) char smem[G1_BOFF + 2 * 24576];
  const int b = blockIdx.x;
  if (b >= 224) {
    for (int it = (b - 224) * 512 + threadIdx.x; it < 2368 * 80; it += 32 * 512) {
      const int k = it / 80, c8 = it - k * 80;
      const u16* src = (k < DF) ? (wd + (size_t)k * DM + c8 * 8) : (wo + (size_t)(k - DF) * DM + c8 * 8);
      const int nb = c8 / 10, r10 = c8 - nb * 10, kb = k >> 3;
      u16* dst = Bt + ((size_t)(nb * 296 + kb) * 640 + (r10 >> 1) * 128 + ((k & 7) ^ ((kb & 1) << 2)) * 16 + (r10 & 1) * 8);
      *(u32x4*)dst = *(const u32x4*)src;
    }
    return;
  }
  const int t = (b & 7) * 28 + (b >> 3);
  const int nblk = t >> 3, mblk = t & 7;
  const int tid = threadIdx.x, wave = tid >> 6, lane = tid & 63;
  const int wm = wave >> 1, wn = wave & 1;
  const int m0 = mblk * 256;
  const bool isqkv = nblk < 10;

  const unsigned a_vo = ((m0 + wave * 32 + (lane >> 3)) * DM + ((lane & 7) ^ (lane >> 4)) * 8) * 2;
  const int ldb = isqkv ? 3 * DM : DF;
  const u16* b_src[3];
#pragma unroll
  for (int i = 0; i < 3; ++i) {
    int kb = wave;
    int nt = i * 4 + (lane >> 4);
    int rr = ((lane >> 1) & 7) ^ ((kb & 1) << 2);
    int k = kb * 8 + rr;
    const u16* base;
    if (isqkv) base = wqkv + nblk * 192 + nt * 16;
    else base = (nt < 6) ? (wg + (nblk - 10) * 96 + nt * 16) : (wu + (nblk - 10) * 96 + (nt - 6) * 16);
    b_src[i] = base + (size_t)k * ldb + (lane & 1) * 8;
  }
  const unsigned lds0 = __builtin_amdgcn_readfirstlane((unsigned)(size_t)smem);
  const unsigned wv = __builtin_amdgcn_readfirstlane(wave);
  auto stage_a = [&](int kt, int g) {
    glds16s(x + g * 8 * DM + kt * 64, a_vo ^ ((g & 1) << 6), lds0 + (kt % 3) * 32768 + (wv * 4 + g) * 1024);
  };
  auto stage_b = [&](int kt, int g) {
    glds16(b_src[g] + (size_t)kt * 64 * ldb, lds0 + G1_BOFF + (kt & 1) * 24576 + (wv * 3 + g) * 1024);
  };
#pragma unroll
  for (int kt = 0; kt < 2; ++kt) {
#pragma unroll
    for (int g = 0; g < 4; ++g) stage_a(kt, g);
#pragma unroll
    for (int g = 0; g < 3; ++g) stage_b(kt, g);
  }

  const int a_off = ((wm * 64 + wn * 32 + (lane & 15)) * 128 + (((lane >> 4) ^ ((lane >> 1) & 7)) << 4)) ^ ((wm & 1) << 6);
  int b_off[2][2];
  {
    int g = lane >> 4, q = (lane & 15) >> 2, p = lane & 3;
    int base = G1_BOFF + g * 3072 + ((g & 1) << 7) + q * 32 + p * 8;
    int t0 = (isqkv ? wn * 6 : wn * 3) * 256, t1 = (isqkv ? wn * 6 + 3 : 6 + wn * 3) * 256;
    if (wm >= 2) { int tt = t0; t0 = t1; t1 = tt; }
#pragma unroll
    for (int kk = 0; kk < 2; ++kk) {
      b_off[kk][0] = base + t0 + (kk ^ (wm & 1)) * 12288;
      b_off[kk][1] = base + t1 + (kk ^ (wm & 1)) * 12288;
    }
  }

  f32x4 acc[4][6];
#pragma unroll
  for (int i = 0; i < 4; ++i)
#pragma unroll
    for (int j = 0; j < 6; ++j) acc[i][j] = (f32x4){0.f, 0.f, 0.f, 0.f};

  bf16x8 af0[4], bf0[6], af1[4], bf1[6];
  auto read_q = [&](bf16x8 (&af)[4], bf16x8 (&bfr)[6], int kt, int kk, int g) {
    const char* sa = smem + (kt % 3) * 32768;
    const char* sb = smem + (kt & 1) * 24576;
    auto rb = [&](int j) {
      s16x4 lo = tr_read(sb + b_off[kk][j / 3] + (j % 3) * 256);
      s16x4 hi = tr_read(sb + (b_off[kk][j / 3] ^ 128) + (j % 3) * 256);
      bfr[j] = __builtin_shufflevector(lo, hi, 0, 1, 2, 3, 4, 5, 6, 7);
    };
    auto ra = [&](int i) { af[i] = *(const bf16x8*)(sa + ((a_off ^ (kk << 6)) ^ ((i >> 1) << 12)) + (i & 1) * 2048); };
    if (g == 0) { ra(0); rb(0); rb(1); }
    if (g == 1) { rb(2); rb(3); }
    if (g == 2) { rb(4); rb(5); }
    if (g == 3) { ra(1); ra(2); ra(3); }
  };
  auto mma_q = [&](bf16x8 (&af)[4], bf16x8 (&bfr)[6], int i) {
#pragma unroll
    for (int j = 0; j < 6; ++j)
      acc[i][j] = __builtin_amdgcn_mfma_f32_16x16x32_bf16(bfr[j], af[i], acc[i][j], 0, 0, 0);
  };

  float rs[2] = {0.f, 0.f}, rq[2] = {0.f, 0.f};
  f32x4 cs[3];
#pragma unroll
  for (int j = 0; j < 3; ++j) cs[j] = (f32x4){0.f, 0.f, 0.f, 0.f};
  const bf16x8 ones = {0x3F80, 0x3F80, 0x3F80, 0x3F80, 0x3F80, 0x3F80, 0x3F80, 0x3F80};
  auto stats_q = [&](bf16x8 (&af)[4], bf16x8 (&bfr)[6], int kk, int g) {
    if (g < 2) {
      const bf8_t w = __builtin_bit_cast(bf8_t, af[g]);
      const bf2_t v[4] = {__builtin_shufflevector(w, w, 0, 1), __builtin_shufflevector(w, w, 2, 3),
                          __builtin_shufflevector(w, w, 4, 5), __builtin_shufflevector(w, w, 6, 7)};
#pragma unroll
      for (int c = 0; c < 4; ++c) {
        rs[g] = __builtin_amdgcn_fdot2_f32_bf16(v[c], __builtin_bit_cast(bf2_t, 0x3F803F80u), rs[g], false);
        rq[g] = __builtin_amdgcn_fdot2_f32_bf16(v[c], v[c], rq[g], false);
      }
    }
    if (g < 3 && kk == 0) cs[g] = __builtin_amdgcn_mfma_f32_16x16x32_bf16(bfr[g], ones, cs[g], 0, 0, 0);
  };

  asm volatile("s_waitcnt vmcnt(7)" ::: "memory");
  barrier_raw();
#pragma unroll
  for (int g = 0; g < 4; ++g) read_q(af0, bf0, 0, 0, g);
#pragma unroll
  for (int kt = 0; kt < 10; ++kt) {
#pragma unroll
    for (int g = 0; g < 4; ++g) {
      __builtin_amdgcn_sched_barrier(0);
      read_q(af1, bf1, kt, 1, g);
      mma_q(af0, bf0, g);
      __builtin_amdgcn_sched_barrier(0);
      stats_q(af0, bf0, 0, g);
      if (g < 2) asm volatile("" : "+v"(rs[g & 1]), "+v"(rq[g & 1]));
      if (kt + 2 < 10) stage_a(kt + 2, g);
    }
    __builtin_amdgcn_sched_barrier(0);
    if (kt + 1 < 10) {
      if (kt + 2 < 10) asm volatile("s_waitcnt vmcnt(4)" ::: "memory");
      else wait_vm0();
      barrier_raw();
    }
#pragma unroll
    for (int g = 0; g < 4; ++g) {
      __builtin_amdgcn_sched_barrier(0);
      if (kt + 1 < 10) read_q(af0, bf0, kt + 1, 0, g);
      mma_q(af1, bf1, g);
      __builtin_amdgcn_sched_barrier(0);
      stats_q(af1, bf1, 1, g);
      if (g < 2) asm volatile("" : "+v"(rs[g & 1]), "+v"(rq[g & 1]));
      if (kt + 2 < 10 && g < 3) stage_b(kt + 2, g);
    }
  }
  __builtin_amdgcn_sched_barrier(0);
  barrier_raw();

  int tz = threadIdx.x;
  asm volatile("" : "+v"(tz));
  const int e_wave = tz >> 6, e_lane = tz & 63, e_wm = e_wave >> 1, e_wn = e_wave & 1;
  const int ln4 = (e_lane >> 4) * 4;
  {
    f32x2* st_row = (f32x2*)(smem + 65536);
    float* st_col = (float*)(smem + 65536 + 4096);
#pragma unroll
    for (int i = 0; i < 2; ++i) {
      float a = rs[i], q = rq[i];
      a += __shfl_xor(a, 16); q += __shfl_xor(q, 16);
      a += __shfl_xor(a, 32); q += __shfl_xor(q, 32);
      if (e_lane < 16) st_row[e_wm * 64 + e_wn * 32 + i * 16 + e_lane] = (f32x2){a, q};
    }
    if ((e_lane & 15) == 0) {
#pragma unroll
      for (int j = 0; j < 3; ++j)
        *(f32x4*)(st_col + ((e_wm & 1) * 2 + e_wn) * 96 + ((e_wm >> 1) * 3 + j) * 16 + ln4) = cs[j];
    }
    barrier_raw();
    float mb[4], inv[4];
#pragma unroll
    for (int i = 0; i < 4; ++i) {
      const f32x2 p0 = st_row[e_wm * 64 + ((i + 2 * e_wn) & 3) * 16 + (e_lane & 15)];
      const float mean = p0[0] * (1.f / DM);
      const float var = fmaxf(p0[1] * (1.f / DM) - mean * mean, 0.f);
      mb[i] = rbf(mean);
      inv[i] = 1.f / rbf(sqrtf(rbf(rbf(var) + 1e-5f)));
    }
#pragma unroll
    for (int j = 0; j < 6; ++j) {
      const int jl = (e_wm >= 2) ? (j + 3) % 6 : j;
      const f32x4 c = *(const f32x4*)(st_col + e_wn * 96 + jl * 16 + ln4) + *(const f32x4*)(st_col + (2 + e_wn) * 96 + jl * 16 + ln4);
#pragma unroll
      for (int i = 0; i < 4; ++i)
#pragma unroll
        for (int r = 0; r < 4; ++r) acc[i][j][r] = __builtin_fmaf(-mb[i], c[r], acc[i][j][r]) * inv[i];
    }
  }
  if (isqkv) {
    const float qscale = 0.22360679774997896f * 1.4426950408889634f;
    char* wtr = smem + e_wave * 512;
#pragma unroll
    for (int j = 0; j < 6; ++j) {
      const int nb = nblk * 192 + (e_wn * 6 + ((e_wm >= 2) ? (j + 3) % 6 : j)) * 16;
      const int sec = nb / DM;
      if (sec < 2) {
        const int rr = nb + ln4 - sec * DM, head = rr / DH, d = rr - head * DH, d4 = d >> 2;
#pragma unroll
        for (int i = 0; i < 4; ++i) {
          const int tok = m0 + e_wm * 64 + ((i + 2 * e_wn) & 3) * 16 + (e_lane & 15);
          f32x4 v = acc[i][j];
          if (sec == 0) {
            u32x2 o = {pack_bf2(v[0] * qscale, v[1] * qscale), pack_bf2(v[2] * qscale, v[3] * qscale)};
            *(u32x2*)(Qg + ((size_t)(head * SEQ + tok) * 32 + d)) = o;
          } else {
            char* kb = (char*)Kf + (size_t)(head * 64 + (tok >> 5)) * 1536;
            const int r = tok & 31;
            unsigned p0 = pack_bf2(v[0], v[1]), p1 = pack_bf2(v[2], v[3]);
            if (d4 < 4) {
              u32x2 o = {p0, p1};
              *(u32x2*)(kb + ((d4 >> 1) * 32 + r) * 16 + (d4 & 1) * 8) = o;
            } else {
              u32x4 o = {p0, p1, 0x00003F80u, 0u};
              *(u32x4*)(kb + 1024 + r * 16) = o;
            }
          }
        }
      } else {
        const int rr = nb + (e_lane & 15) - 2 * DM, head = rr / DH, d = rr - head * DH;
        const int g = e_lane >> 4;
#pragma unroll
        for (int i = 0; i < 4; ++i) {
          f32x4 v = acc[i][j];
          u32x2 w = {pack_bf2(v[0], v[1]), pack_bf2(v[2], v[3])};
          *(u32x2*)(wtr + (e_lane & 15) * 32 + g * 8) = w;
          s16x4 tv = tr_read(wtr + g * 128 + ((e_lane & 15) >> 2) * 32 + (e_lane & 3) * 8);
          const int s = (m0 + e_wm * 64 + ((i + 2 * e_wn) & 3) * 16) >> 4;
          u16* vb = Vf + ((size_t)(head * 128 + s) * 48 + (g & 1) * 24 + d) * 8 + (g >> 1) * 4;
          *(s16x4*)vb = tv;
          if (d == DH - 1) { s16x4 one = {0x3F80, 0x3F80, 0x3F80, 0x3F80}; *(s16x4*)(vb + 8) = one; }
        }
      }
    }
  } else {
#pragma unroll
    for (int j = 0; j < 3; ++j) {
      const int f = (nblk - 10) * 96 + (e_wn * 3 + j) * 16 + ln4;
#pragma unroll
      for (int i = 0; i < 4; ++i) {
        const int tok = m0 + e_wm * 64 + ((i + 2 * e_wn) & 3) * 16 + (e_lane & 15);
        float hv[4];
#pragma unroll
        for (int r = 0; r < 4; ++r) {
          const float g = (e_wm >= 2) ? acc[i][j + 3][r] : acc[i][j][r], u = (e_wm >= 2) ? acc[i][j][r] : acc[i][j + 3][r];
          const float e = __builtin_amdgcn_exp2f(g * __builtin_fmaf(g * g, -0.10294324f, -2.30220819f));
          hv[r] = (g * u) * __builtin_amdgcn_rcpf(1.f + e);
        }
        u32x2 o = {pack_bf2(hv[0], hv[1]), pack_bf2(hv[2], hv[3])};
        *(u32x2*)(hbuf + (size_t)tok * DF + f) = o;
      }
    }
  }
}

constexpr int AT_STAGE = 24576;
constexpr float AT_BIAS = 8.f;
#define MFMA32(a, b, c) __builtin_amdgcn_mfma_f32_32x32x16_bf16((a), (b), (c), 0, 0, 0)
#define MFMA16(a, b, c) __builtin_amdgcn_mfma_f32_16x16x32_bf16((a), (b), (c), 0, 0, 0)

DI void attn_role(char* smem, int b, const u16* __restrict__ Qg, const u16* __restrict__ Kf,
                  const u16* __restrict__ Vf, u16* __restrict__ obuf) {
  const int head = (b & 7) * 4 + ((b >> 3) & 3), qb = b >> 5;
  const int tid = threadIdx.x, wave = tid >> 6, lane = tid & 63;
  const int r = lane & 31, h = lane >> 5;
  const int q = qb * 256 + wave * 32 + r;

  const unsigned lds0 = __builtin_amdgcn_readfirstlane((unsigned)(size_t)smem);
  const unsigned wv = __builtin_amdgcn_readfirstlane(wave);
  const char* gsrc = ((wv < 4) ? (const char*)Kf + wv * 3072 : (const char*)Vf + (wv - 4) * 3072) +
                     (size_t)head * 98304 + lane * 16;
  auto stage = [&](unsigned boff, int tile) {
#pragma unroll
    for (int i = 0; i < 3; ++i) glds16(gsrc + tile * 12288 + i * 1024, lds0 + boff + (wv * 3 + i) * 1024);
  };
  const u16* qrow = Qg + (size_t)(head * SEQ + q) * 32;
  bf16x8 q1 = *(const bf16x8*)(qrow + 8 * h);
  u32x4 q2 = {0u, 0u, 0u, 0u};
  {
    u32x2 t2 = *(const u32x2*)(qrow + 16);
    if (h == 0) { q2[0] = t2[0]; q2[1] = t2[1]; }
  }
  stage(0, 0);
  stage(AT_STAGE, 1);

  const int k1_off = lane * 16, k2_off = 1024 + r * 16;
  const int v_off = 12288 + (h * 24 + (r < 23 ? r : 23)) * 16;

  f32x16 o, s0, s1;
  const f32x16 zero = {0.f, 0.f, 0.f, 0.f, 0.f, 0.f, 0.f, 0.f, 0.f, 0.f, 0.f, 0.f, 0.f, 0.f, 0.f, 0.f};
  o = zero;
  float mref;

  asm volatile("s_waitcnt vmcnt(3)" ::: "memory");
  barrier_raw();
  {
    s0 = MFMA32(*(const bf16x8*)(smem + k1_off), q1, zero);
    s0 = MFMA32(*(const bf16x8*)(smem + k2_off), __builtin_bit_cast(bf16x8, q2), s0);
    s1 = MFMA32(*(const bf16x8*)(smem + 1536 + k1_off), q1, zero);
    s1 = MFMA32(*(const bf16x8*)(smem + 1536 + k2_off), __builtin_bit_cast(bf16x8, q2), s1);
    float mx = fmaxf(s0[0], s1[0]);
#pragma unroll
    for (int i = 1; i < 16; ++i) mx = fmaxf(mx, fmaxf(s0[i], s1[i]));
    mref = rbf(fmaxf(mx, __shfl_xor(mx, 32)) + AT_BIAS);
    if (h == 0) q2[2] = pack_bf2(-mref, 0.f);
#pragma unroll
    for (int i = 0; i < 16; ++i) { s0[i] -= mref; s1[i] -= mref; }
  }
  unsigned cur = 0;
  for (int tile = 0; tile < 8; ++tile) {
    const unsigned nxt = (cur == 2 * AT_STAGE) ? 0 : cur + AT_STAGE;
#pragma unroll
    for (int g = 0; g < 4; ++g) {
      f32x16 n0, n1;
      const char* kb;
      if (g == 3) {
        if (tile < 7) {
          wait_vm0();
          barrier_raw();
          if (tile < 6) stage((nxt == 2 * AT_STAGE) ? 0 : nxt + AT_STAGE, tile + 2);
        }
        kb = smem + nxt;
      } else {
        kb = smem + cur + (g + 1) * 3072;
      }
      n0 = MFMA32(*(const bf16x8*)(kb + k1_off), q1, zero);
      n0 = MFMA32(*(const bf16x8*)(kb + k2_off), __builtin_bit_cast(bf16x8, q2), n0);
      n1 = MFMA32(*(const bf16x8*)(kb + 1536 + k1_off), q1, zero);
      n1 = MFMA32(*(const bf16x8*)(kb + 1536 + k2_off), __builtin_bit_cast(bf16x8, q2), n1);
      unsigned p[16];
#pragma unroll
      for (int i = 0; i < 8; ++i) {
        p[i] = pack_bf2(__builtin_amdgcn_exp2f(s0[2 * i]), __builtin_amdgcn_exp2f(s0[2 * i + 1]));
        p[8 + i] = pack_bf2(__builtin_amdgcn_exp2f(s1[2 * i]), __builtin_amdgcn_exp2f(s1[2 * i + 1]));
      }
      unsigned orr = p[0];
#pragma unroll
      for (int i = 1; i < 16; ++i) orr |= p[i];
      if (__builtin_amdgcn_ballot_w64((orr & 0x40004000u) != 0) != 0) {
        const char* kc = smem + cur + g * 3072;
        float mx = -3.0e38f;
#pragma unroll
        for (int hf = 0; hf < 2; ++hf) {
          f32x16 t = MFMA32(*(const bf16x8*)(kc + hf * 1536 + k1_off), q1, zero);
          t = MFMA32(*(const bf16x8*)(kc + hf * 1536 + k2_off), __builtin_bit_cast(bf16x8, q2), t);
#pragma unroll
          for (int i = 0; i < 16; ++i) mx = fmaxf(mx, t[i]);
          __builtin_amdgcn_sched_barrier(0);
        }
        mx = fmaxf(mx, __shfl_xor(mx, 32));
        const float mnew = rbf(mref + fmaxf(mx + AT_BIAS, 0.f));
        const float delta = mnew - mref;
        const float alpha = __builtin_amdgcn_exp2f(-delta);
        mref = mnew;
        if (h == 0) q2[2] = pack_bf2(-mref, 0.f);
#pragma unroll
        for (int i = 0; i < 16; ++i) { n0[i] -= delta; n1[i] -= delta; o[i] *= alpha; }
#pragma unroll
        for (int hf = 0; hf < 2; ++hf) {
          __builtin_amdgcn_sched_barrier(0);
          f32x16 t = MFMA32(*(const bf16x8*)(kc + hf * 1536 + k1_off), q1, zero);
          t = MFMA32(*(const bf16x8*)(kc + hf * 1536 + k2_off), __builtin_bit_cast(bf16x8, q2), t);
#pragma unroll
          for (int i = 0; i < 8; ++i)
            p[hf * 8 + i] = pack_bf2(__builtin_amdgcn_exp2f(t[2 * i]), __builtin_amdgcn_exp2f(t[2 * i + 1]));
        }
      }
#pragma unroll
      for (int s = 0; s < 4; ++s) {
        u32x4 pv = {p[4 * s], p[4 * s + 1], p[4 * s + 2], p[4 * s + 3]};
        bf16x8 vf = *(const bf16x8*)(smem + cur + (g * 4 + s) * 768 + v_off);
        o = MFMA32(vf, __builtin_bit_cast(bf16x8, pv), o);
      }
      s0 = n0; s1 = n1;
    }
    cur = nxt;
  }
  const float l = __shfl(o[8], r + 32);
  const float inv = 1.f / l;
  auto ostore = [&](int d, u32x2 w) {
    const int col = head * DH + d;
    *(u32x2*)(obuf + ((size_t)((q >> 4) * 20 + (col >> 5)) * 64 + ((col >> 3) & 3) * 16 + (q & 15)) * 8 + (col & 7)) = w;
  };
  {
    u32x2 w0 = {pack_bf2(o[0] * inv, o[1] * inv), pack_bf2(o[2] * inv, o[3] * inv)};
    u32x2 w1 = {pack_bf2(o[4] * inv, o[5] * inv), pack_bf2(o[6] * inv, o[7] * inv)};
    ostore(4 * h, w0);
    ostore(4 * h + 8, w1);
    if (h == 0) {
      u32x2 w2 = {pack_bf2(o[8] * inv, o[9] * inv), pack_bf2(o[10] * inv, o[11] * inv)};
      ostore(16, w2);
    }
  }
}

constexpr int GS_STAGE = 18432;
DI void down_role(char* smem, int b, const u16* __restrict__ hbuf, const u16* __restrict__ Bt,
                  const u16* __restrict__ x, float* __restrict__ y) {
  constexpr int NSLOT = 4, NST = DF / 64;
  const int t = (b & 7) * 32 + (b >> 3);
  const int nblk = t & 7, mblk = t >> 3;
  const int tid = threadIdx.x, wave = tid >> 6, lane = tid & 63;
  const int m0 = mblk * 64, n0 = nblk * 80;
  const unsigned lds0 = __builtin_amdgcn_readfirstlane((unsigned)(size_t)smem);
  const unsigned wv = __builtin_amdgcn_readfirstlane(wave);

  unsigned a_vo, b_vo;
  {
    int row = wave * 8 + (lane >> 3);
    a_vo = ((m0 + row) * DF + ((lane & 7) ^ ((row >> 1) & 7)) * 8) * 2;
    b_vo = (nblk * 296 + wave) * 1280 + lane * 16;
  }
  auto stage = [&](int st) {
    unsigned sa = lds0 + (st & (NSLOT - 1)) * GS_STAGE;
    glds16s(hbuf + st * 64, a_vo, sa + wv * 1024);
    glds16s(Bt + st * 5120, b_vo, sa + 8192 + wv * 1280);
    if (lane < 16) glds16s(Bt + st * 5120 + 512, b_vo, sa + 8192 + wv * 1280 + 1024);
  };
#pragma unroll
  for (int st = 0; st < NSLOT - 1; ++st) stage(st);
  const int mh = wave & 1, kk = (wave >> 1) & 1;
  const unsigned par = wv >> 2;
  const int a_off = (mh * 32 + (lane & 15)) * 128 + (((kk * 4 + (lane >> 4)) ^ ((lane >> 1) & 7)) << 4);
  int b_off;
  {
    int g = lane >> 4, q = (lane & 15) >> 2, p = lane & 3;
    b_off = 8192 + (kk * 4 + g) * 1280 + ((g & 1) << 7) + q * 32 + p * 8;
  }
  f32x4 acc[2][5];
#pragma unroll
  for (int i = 0; i < 2; ++i)
#pragma unroll
    for (int j = 0; j < 5; ++j) acc[i][j] = (f32x4){0.f, 0.f, 0.f, 0.f};

  auto step = [&](int st, auto waitc) {
    asm volatile("s_waitcnt vmcnt(%0)" :: "i"(decltype(waitc)::value) : "memory");
    barrier_raw();
    if (st + NSLOT - 1 < NST) stage(st + NSLOT - 1);
    if ((unsigned)(st & 1) == par) {
      const char* sbase = smem + (st & (NSLOT - 1)) * GS_STAGE;
      bf16x8 af0 = *(const bf16x8*)(sbase + a_off);
      bf16x8 af1 = *(const bf16x8*)(sbase + a_off + 2048);
      bf16x8 bfr[5];
#pragma unroll
      for (int j = 0; j < 5; ++j) {
        s16x4 lo = tr_read(sbase + b_off + j * 256);
        s16x4 hi = tr_read(sbase + (b_off ^ 128) + j * 256);
        bfr[j] = __builtin_shufflevector(lo, hi, 0, 1, 2, 3, 4, 5, 6, 7);
      }
#pragma unroll
      for (int j = 0; j < 5; ++j) {
        acc[0][j] = MFMA16(bfr[j], af0, acc[0][j]);
        acc[1][j] = MFMA16(bfr[j], af1, acc[1][j]);
      }
    }
  };
  for (int st = 0; st < NST - (NSLOT - 2); ++st) step(st, IC<3 * (NSLOT - 2)>{});
  static_for<NSLOT - 2>([&](auto i) { step(NST - (NSLOT - 2) + i.value, IC<3 * (NSLOT - 3 - i.value)>{}); });
  barrier_raw();
  if (wv >= 2) {
    float* red = (float*)smem + ((wave - 2) * 64 + lane) * 40;
#pragma unroll
    for (int i = 0; i < 2; ++i)
#pragma unroll
      for (int j = 0; j < 5; ++j) *(f32x4*)(red + (i * 5 + j) * 4) = acc[i][j];
  }
  barrier_raw();
  if (wv < 2) {
#pragma unroll
    for (int i = 0; i < 2; ++i) {
      const size_t gi = (size_t)(m0 + mh * 32 + i * 16 + (lane & 15)) * DM + n0 + (lane >> 4) * 4;
#pragma unroll
      for (int j = 0; j < 5; ++j) {
        f32x4 s = acc[i][j];
#pragma unroll
        for (int w = 0; w < 3; ++w) s += *(const f32x4*)((float*)smem + ((w * 2 + mh) * 64 + lane) * 40 + (i * 5 + j) * 4);
        u32x2 xv = *(const u32x2*)(x + gi + j * 16);
        s[0] += bf_lo(xv[0]); s[1] += bf_hi(xv[0]); s[2] += bf_lo(xv[1]); s[3] += bf_hi(xv[1]);
        *(f32x4*)(y + ((size_t)(t * 20 + mh * 10 + i * 5 + j) * 64 + lane) * 4) = s;
      }
    }
  }
}

__global__ __launch_bounds__(512, 4) void k2_kernel(const u16* __restrict__ Qg, const u16* __restrict__ Kf,
                                                    const u16* __restrict__ Vf, u16* __restrict__ obuf,
                                                    const u16* __restrict__ hbuf, const u16* __restrict__ Bt,
                                                    const u16* __restrict__ x, float* __restrict__ y) {
  __shared__ __attribute__((aligned(16))) char smem[3 * AT_STAGE];
  const int b = blockIdx.x;
  if (b < 256) attn_role(smem, b, Qg, Kf, Vf, obuf);
  else down_role(smem, b - 256, hbuf, Bt, x, y);
}

__global__ __launch_bounds__(512, 2) void k3_kernel(const u16* __restrict__ obuf, const u16* __restrict__ Bt,
                                                    const float* __restrict__ y, u16* __restrict__ out) {
  __shared__ __attribute__((aligned(16))) char smem[80 * 1280];
  const int b = blockIdx.x;
  const int t = (b & 7) * 32 + (b >> 3);
  const int nblk = t & 7, mblk = t >> 3;
  const int tid = threadIdx.x, wave = tid >> 6, lane = tid & 63;
  const int m0 = mblk * 64, n0 = nblk * 80;
  const unsigned lds0 = __builtin_amdgcn_readfirstlane((unsigned)(size_t)smem);
  const unsigned wv = __builtin_amdgcn_readfirstlane(wave);
  {
    const unsigned b_vo = (nblk * 296 + DF / 8) * 1280 + wave * 1024 + lane * 16;
#pragma unroll
    for (int i = 0; i < 13; ++i)
      if (i < 12 || wv < 4) glds16s(Bt + i * 4096, b_vo, lds0 + (i * 8 + wv) * 1024);
  }
  const int mh = wave & 1, ks = wave >> 1;
  bf16x8 af[5][2];
  {
    const u16* ap = obuf + ((size_t)((mblk * 4 + mh * 2) * 20 + ks) * 64 + lane) * 8;
#pragma unroll
    for (int s = 0; s < 5; ++s) {
      af[s][0] = *(const bf16x8*)(ap + s * 4 * 512);
      af[s][1] = *(const bf16x8*)(ap + 20 * 512 + s * 4 * 512);
    }
  }
  f32x4 yv[3];
  {
    const float* yp = y + ((size_t)(t * 20 + wave) * 64 + lane) * 4;
    yv[0] = *(const f32x4*)(yp);
    yv[1] = *(const f32x4*)(yp + 8 * 256);
    yv[2] = *(const f32x4*)(yp + (wv < 4 ? 16 : 8) * 256);
  }
  f32x4 acc[2][5];
#pragma unroll
  for (int i = 0; i < 2; ++i)
#pragma unroll
    for (int j = 0; j < 5; ++j) acc[i][j] = (f32x4){0.f, 0.f, 0.f, 0.f};
  int b_off;
  {
    int g = lane >> 4, q = (lane & 15) >> 2, p = lane & 3;
    b_off = (ks * 4 + g) * 1280 + ((g & 1) << 7) + q * 32 + p * 8;
  }
  wait_vm0();
  barrier_raw();
#pragma unroll
  for (int s = 0; s < 5; ++s) {
    bf16x8 bfr[5];
#pragma unroll
    for (int j = 0; j < 5; ++j) {
      s16x4 lo = tr_read(smem + b_off + s * 20480 + j * 256);
      s16x4 hi = tr_read(smem + (b_off ^ 128) + s * 20480 + j * 256);
      bfr[j] = __builtin_shufflevector(lo, hi, 0, 1, 2, 3, 4, 5, 6, 7);
    }
#pragma unroll
    for (int j = 0; j < 5; ++j) {
      acc[0][j] = MFMA16(bfr[j], af[s][0], acc[0][j]);
      acc[1][j] = MFMA16(bfr[j], af[s][1], acc[1][j]);
    }
  }
  barrier_raw();
  {
    f32x4* red = (f32x4*)smem + (ks * 20 + mh * 10) * 64 + lane;
#pragma unroll
    for (int i = 0; i < 2; ++i)
#pragma unroll
      for (int j = 0; j < 5; ++j) red[(i * 5 + j) * 64] = acc[i][j];
  }
  barrier_raw();
#pragma unroll
  for (int u = 0; u < 3; ++u) {
    const int tile = wave + u * 8;
    if (u < 2 || wv < 4) {
      f32x4 s = yv[u];
#pragma unroll
      for (int w = 0; w < 4; ++w) s += ((const f32x4*)smem)[(w * 20 + tile) * 64 + lane];
      const int rt = tile / 5, j = tile - rt * 5;
      u32x2 o = {pack_bf2(s[0], s[1]), pack_bf2(s[2], s[3])};
      *(u32x2*)(out + (size_t)(m0 + rt * 16 + (lane & 15)) * DM + n0 + j * 16 + (lane >> 4) * 4) = o;
    }
  }
}

extern "C" void kernel_launch(void* const* d_in, const int* in_sizes, int n_in, void* d_out, int out_size,
                              void* d_ws, size_t ws_size, hipStream_t stream) {
  const u16* x = (const u16*)d_in[0];
  const u16* wqkv = (const u16*)d_in[1];
  const u16* wo = (const u16*)d_in[2];
  const u16* wg = (const u16*)d_in[4];
  const u16* wu = (const u16*)d_in[5];
  const u16* wd = (const u16*)d_in[6];
  char* ws = (char*)d_ws;
  u16* Qg = (u16*)(ws + (4u << 20));
  u16* Kf = (u16*)(ws + (8u << 20));
  u16* Vf = (u16*)(ws + (12u << 20));
  u16* hb = (u16*)(ws + (16u << 20));
  u16* ob = (u16*)(ws + (24u << 20));
  float* yb = (float*)(ws + (28u << 20));
  u16* Bt = (u16*)(ws + (34u << 20));
  gemm1_kernel<<<256, 512, 0, stream>>>(x, wqkv, wg, wu, Qg, Kf, Vf, hb, wd, wo, Bt);
  k2_kernel<<<512, 512, 0, stream>>>(Qg, Kf, Vf, ob, hb, Bt, x, yb);
  k3_kernel<<<256, 512, 0, stream>>>(ob, Bt, yb, (u16*)d_out);
}
```

```cpp
#include <hip/hip_runtime.h>
#include <stdint.h>

typedef __attribute__((ext_vector_type(8))) short bf16x8;
typedef __attribute__((ext_vector_type(4))) short s16x4;
typedef __attribute__((ext_vector_type(4))) float f32x4;
typedef __attribute__((ext_vector_type(2))) float f32x2;
typedef __attribute__((ext_vector_type(16))) float f32x16;
typedef __attribute__((ext_vector_type(2))) __bf16 bf2_t;
typedef __attribute__((ext_vector_type(8))) __bf16 bf8_t;
typedef __attribute__((ext_vector_type(4))) unsigned u32x4;
typedef __attribute__((ext_vector_type(2))) unsigned u32x2;
typedef unsigned short u16;
typedef __attribute__((address_space(3))) s16x4 lds_s16x4;

#define DI __device__ __forceinline__

constexpr int SEQ = 2048, DM = 640, NH = 32, DH = 20, DF = 1728;

DI unsigned pack_bf2(float a, float b) {
  f32x2 v = {a, b};
  return __builtin_bit_cast(unsigned, __builtin_convertvector(v, bf2_t));
}
DI float bf_lo(unsigned u) { return __uint_as_float(u << 16); }
DI float bf_hi(unsigned u) { return __uint_as_float(u & 0xffff0000u); }
DI float rbf(float x) { return bf_lo(pack_bf2(x, 0.f)); }

DI void glds16(const void* g, unsigned lds_addr) {
  unsigned keep;
  asm volatile("s_mov_b32 %0, m0\n\ts_mov_b32 m0, %2\n\ts_nop 0\n\tglobal_load_lds_dwordx4 %1, off\n\ts_mov_b32 m0, %0"
               : "=&s"(keep) : "v"(g), "s"(lds_addr) : "memory");
}
DI void glds16s(const void* sbase, unsigned voff, unsigned lds_addr) {
  unsigned keep;
  asm volatile("s_mov_b32 %0, m0\n\ts_mov_b32 m0, %3\n\ts_nop 0\n\tglobal_load_lds_dwordx4 %1, %2\n\ts_mov_b32 m0, %0"
               : "=&s"(keep) : "v"(voff), "s"(sbase), "s"(lds_addr) : "memory");
}
DI void wait_vm0() { asm volatile("s_waitcnt vmcnt(0)" ::: "memory"); }
DI void barrier_raw() {
  asm volatile("s_waitcnt lgkmcnt(0)" ::: "memory");
  __builtin_amdgcn_s_barrier();
  asm volatile("" ::: "memory");
}
template <int V> struct IC { static constexpr int value = V; };
template <int N, int I = 0, class F> DI void static_for(F&& f) {
  if constexpr (I < N) { f(IC<I>{}); static_for<N, I + 1>(f); }
}
DI s16x4 tr_read(const char* lds) {
  return __builtin_amdgcn_ds_read_tr16_b64_v4i16((lds_s16x4*)lds);
}

constexpr int G1_BOFF = 3 * 32768;
__global__ __launch_bounds__(512, 2) void gemm1_kernel(const u16* __restrict__ x, const u16* __restrict__ wqkv,
                                                       const u16* __restrict__ wg, const u16* __restrict__ wu,
                                                       u16* __restrict__ Qg, u16* __restrict__ Kf,
                                                       u16* __restrict__ Vf, u16* __restrict__ hbuf,
                                                       const u16* __restrict__ wd, const u16* __restrict__ wo,
                                                       u16* __restrict__ Bt, u16* __restrict__ Bf) {
  __shared__ __attribute__((aligned(16))) char smem[G1_BOFF + 2 * 24576];
  const int b = blockIdx.x;
  if (b >= 224) {
    const int tsp = (b - 224) * 512 + threadIdx.x;
    {
      u32x4 v[4];
#pragma unroll
      for (int u = 0; u < 4; ++u) {
        int it = tsp + u * 16384;
        if (it >= 640 * 80) it = 0;
        const int k = it / 80, c8 = it - k * 80;
        v[u] = *(const u32x4*)(wo + (size_t)k * DM + c8 * 8);
      }
#pragma unroll
      for (int u = 0; u < 4; ++u) {
        const int it = tsp + u * 16384;
        const int k = it / 80, c8 = it - k * 80;
        const int nb = c8 / 10, r10 = c8 - nb * 10, kb = k >> 3;
        u16* dst = Bt + ((size_t)(nb * 80 + kb) * 640 + (r10 >> 1) * 128 + ((k & 7) ^ ((kb & 1) << 2)) * 16 + (r10 & 1) * 8);
        if (it < 640 * 80) *(u32x4*)dst = v[u];
      }
    }
    for (int it = tsp; it < 216 * 640; it += 16384) {
      const int kb = it / 640, n = it - kb * 640;
      const u16* src = wd + (size_t)kb * 8 * DM + n;
      unsigned e[8];
#pragma unroll
      for (int j = 0; j < 8; ++j) e[j] = src[j * DM];
      const int nb = n / 80, r = n - nb * 80;
      u32x4 w = {e[0] | (e[1] << 16), e[2] | (e[3] << 16), e[4] | (e[5] << 16), e[6] | (e[7] << 16)};
      *(u32x4*)(Bf + ((size_t)((nb * 54 + (kb >> 2)) * 5 + (r >> 4)) * 64 + (kb & 3) * 16 + (r & 15)) * 8) = w;
    }
    return;
  }
  const int t = (b & 7) * 28 + (b >> 3);
  const int nblk = t >> 3, mblk = t & 7;
  const int tid = threadIdx.x, wave = tid >> 6, lane = tid & 63;
  const int wm = wave >> 1, wn = wave & 1;
  const int m0 = mblk * 256;
  const bool isqkv = nblk < 10;

  const unsigned a_vo = ((m0 + wave * 32 + (lane >> 3)) * DM + ((lane & 7) ^ (lane >> 4)) * 8) * 2;
  const int ldb = isqkv ? 3 * DM : DF;
  const u16* b_src[3];
#pragma unroll
  for (int i = 0; i < 3; ++i) {
    int kb = wave;
    int nt = i * 4 + (lane >> 4);
    int rr = ((lane >> 1) & 7) ^ ((kb & 1) << 2);
    int k = kb * 8 + rr;
    const u16* base;
    if (isqkv) base = wqkv + nblk * 192 + nt * 16;
    else base = (nt < 6) ? (wg + (nblk - 10) * 96 + nt * 16) : (wu + (nblk - 10) * 96 + (nt - 6) * 16);
    b_src[i] = base + (size_t)k * ldb + (lane & 1) * 8;
  }
  const unsigned lds0 = __builtin_amdgcn_readfirstlane((unsigned)(size_t)smem);
  const unsigned wv = __builtin_amdgcn_readfirstlane(wave);
  auto stage_a = [&](int kt, int g) {
    glds16s(x + g * 8 * DM + kt * 64, a_vo ^ ((g & 1) << 6), lds0 + (kt % 3) * 32768 + (wv * 4 + g) * 1024);
  };
  auto stage_b = [&](int kt, int g) {
    glds16(b_src[g] + (size_t)kt * 64 * ldb, lds0 + G1_BOFF + (kt & 1) * 24576 + (wv * 3 + g) * 1024);
  };
#pragma unroll
  for (int kt = 0; kt < 2; ++kt) {
#pragma unroll
    for (int g = 0; g < 4; ++g) stage_a(kt, g);
#pragma unroll
    for (int g = 0; g < 3; ++g) stage_b(kt, g);
  }

  const int a_off = ((wm * 64 + wn * 32 + (lane & 15)) * 128 + (((lane >> 4) ^ ((lane >> 1) & 7)) << 4)) ^ ((wm & 1) << 6);
  int b_off[2][2];
  {
    int g = lane >> 4, q = (lane & 15) >> 2, p = lane & 3;
    int base = G1_BOFF + g * 3072 + ((g & 1) << 7) + q * 32 + p * 8;
    int t0 = (isqkv ? wn * 6 : wn * 3) * 256, t1 = (isqkv ? wn * 6 + 3 : 6 + wn * 3) * 256;
    if (wm >= 2) { int tt = t0; t0 = t1; t1 = tt; }
#pragma unroll
    for (int kk = 0; kk < 2; ++kk) {
      b_off[kk][0] = base + t0 + (kk ^ (wm & 1)) * 12288;
      b_off[kk][1] = base + t1 + (kk ^ (wm & 1)) * 12288;
    }
  }

  f32x4 acc[4][6];
#pragma unroll
  for (int i = 0; i < 4; ++i)
#pragma unroll
    for (int j = 0; j < 6; ++j) acc[i][j] = (f32x4){0.f, 0.f, 0.f, 0.f};

  bf16x8 af0[4], bf0[6], af1[4], bf1[6];
  auto read_q = [&](bf16x8 (&af)[4], bf16x8 (&bfr)[6], int kt, int kk, int g) {
    const char* sa = smem + (kt % 3) * 32768;
    const char* sb = smem + (kt & 1) * 24576;
    auto rb = [&](int j) {
      s16x4 lo = tr_read(sb + b_off[kk][j / 3] + (j % 3) * 256);
      s16x4 hi = tr_read(sb + (b_off[kk][j / 3] ^ 128) + (j % 3) * 256);
      bfr[j] = __builtin_shufflevector(lo, hi, 0, 1, 2, 3, 4, 5, 6, 7);
    };
    auto ra = [&](int i) { af[i] = *(const bf16x8*)(sa + ((a_off ^ (kk << 6)) ^ ((i >> 1) << 12)) + (i & 1) * 2048); };
    if (g == 0) { ra(0); rb(0); rb(1); }
    if (g == 1) { rb(2); rb(3); }
    if (g == 2) { rb(4); rb(5); }
    if (g == 3) { ra(1); ra(2); ra(3); }
  };
  auto mma_q = [&](bf16x8 (&af)[4], bf16x8 (&bfr)[6], int i) {
#pragma unroll
    for (int j = 0; j < 6; ++j)
      acc[i][j] = __builtin_amdgcn_mfma_f32_16x16x32_bf16(bfr[j], af[i], acc[i][j], 0, 0, 0);
  };

  float rs[2] = {0.f, 0.f}, rq[2] = {0.f, 0.f};
  f32x4 cs[3];
#pragma unroll
  for (int j = 0; j < 3; ++j) cs[j] = (f32x4){0.f, 0.f, 0.f, 0.f};
  const bf16x8 ones = {0x3F80, 0x3F80, 0x3F80, 0x3F80, 0x3F80, 0x3F80, 0x3F80, 0x3F80};
  auto stats_q = [&](bf16x8 (&af)[4], bf16x8 (&bfr)[6], int kk, int g) {
    if (g < 2) {
      const bf8_t w = __builtin_bit_cast(bf8_t, af[g]);
      const bf2_t v[4] = {__builtin_shufflevector(w, w, 0, 1), __builtin_shufflevector(w, w, 2, 3),
                          __builtin_shufflevector(w, w, 4, 5), __builtin_shufflevector(w, w, 6, 7)};
#pragma unroll
      for (int c = 0; c < 4; ++c) {
        rs[g] = __builtin_amdgcn_fdot2_f32_bf16(v[c], __builtin_bit_cast(bf2_t, 0x3F803F80u), rs[g], false);
        rq[g] = __builtin_amdgcn_fdot2_f32_bf16(v[c], v[c], rq[g], false);
      }
    }
    if (g < 3 && kk == 0) cs[g] = __builtin_amdgcn_mfma_f32_16x16x32_bf16(bfr[g], ones, cs[g], 0, 0, 0);
  };

  asm volatile("s_waitcnt vmcnt(7)" ::: "memory");
  barrier_raw();
#pragma unroll
  for (int g = 0; g < 4; ++g) read_q(af0, bf0, 0, 0, g);
#pragma unroll
  for (int kt = 0; kt < 10; ++kt) {
#pragma unroll
    for (int g = 0; g < 4; ++g) {
      __builtin_amdgcn_sched_barrier(0);
      read_q(af1, bf1, kt, 1, g);
      mma_q(af0, bf0, g);
      __builtin_amdgcn_sched_barrier(0);
      stats_q(af0, bf0, 0, g);
      if (g < 2) asm volatile("" : "+v"(rs[g & 1]), "+v"(rq[g & 1]));
      if (kt + 2 < 10) stage_a(kt + 2, g);
    }
    __builtin_amdgcn_sched_barrier(0);
    if (kt + 1 < 10) {
      if (kt + 2 < 10) asm volatile("s_waitcnt vmcnt(4)" ::: "memory");
      else wait_vm0();
      barrier_raw();
    }
#pragma unroll
    for (int g = 0; g < 4; ++g) {
      __builtin_amdgcn_sched_barrier(0);
      if (kt + 1 < 10) read_q(af0, bf0, kt + 1, 0, g);
      mma_q(af1, bf1, g);
      __builtin_amdgcn_sched_barrier(0);
      stats_q(af1, bf1, 1, g);
      if (g < 2) asm volatile("" : "+v"(rs[g & 1]), "+v"(rq[g & 1]));
      if (kt + 2 < 10 && g < 3) stage_b(kt + 2, g);
    }
  }
  __builtin_amdgcn_sched_barrier(0);
  barrier_raw();

  int tz = threadIdx.x;
  asm volatile("" : "+v"(tz));
  const int e_wave = tz >> 6, e_lane = tz & 63, e_wm = e_wave >> 1, e_wn = e_wave & 1;
  const int ln4 = (e_lane >> 4) * 4;
  {
    f32x2* st_row = (f32x2*)(smem + 65536);
    float* st_col = (float*)(smem + 65536 + 4096);
#pragma unroll
    for (int i = 0; i < 2; ++i) {
      float a = rs[i], q = rq[i];
      a += __shfl_xor(a, 16); q += __shfl_xor(q, 16);
      a += __shfl_xor(a, 32); q += __shfl_xor(q, 32);
      if (e_lane < 16) st_row[e_wm * 64 + e_wn * 32 + i * 16 + e_lane] = (f32x2){a, q};
    }
    if ((e_lane & 15) == 0) {
#pragma unroll
      for (int j = 0; j < 3; ++j)
        *(f32x4*)(st_col + ((e_wm & 1) * 2 + e_wn) * 96 + ((e_wm >> 1) * 3 + j) * 16 + ln4) = cs[j];
    }
    barrier_raw();
    float mb[4], inv[4];
#pragma unroll
    for (int i = 0; i < 4; ++i) {
      const f32x2 p0 = st_row[e_wm * 64 + ((i + 2 * e_wn) & 3) * 16 + (e_lane & 15)];
      const float mean = p0[0] * (1.f / DM);
      const float var = fmaxf(p0[1] * (1.f / DM) - mean * mean, 0.f);
      mb[i] = rbf(mean);
      inv[i] = 1.f / rbf(sqrtf(rbf(rbf(var) + 1e-5f)));
    }
#pragma unroll
    for (int j = 0; j < 6; ++j) {
      const int jl = (e_wm >= 2) ? (j + 3) % 6 : j;
      const f32x4 c = *(const f32x4*)(st_col + e_wn * 96 + jl * 16 + ln4) + *(const f32x4*)(st_col + (2 + e_wn) * 96 + jl * 16 + ln4);
#pragma unroll
      for (int i = 0; i < 4; ++i)
#pragma unroll
        for (int r = 0; r < 4; ++r) acc[i][j][r] = __builtin_fmaf(-mb[i], c[r], acc[i][j][r]) * inv[i];
    }
  }
  if (isqkv) {
    const float qscale = 0.22360679774997896f * 1.4426950408889634f;
    char* wtr = smem + e_wave * 512;
#pragma unroll
    for (int j = 0; j < 6; ++j) {
      const int nb = nblk * 192 + (e_wn * 6 + ((e_wm >= 2) ? (j + 3) % 6 : j)) * 16;
      const int sec = nb / DM;
      if (sec < 2) {
        const int rr = nb + ln4 - sec * DM, head = rr / DH, d = rr - head * DH, d4 = d >> 2;
#pragma unroll
        for (int i = 0; i < 4; ++i) {
          const int tok = m0 + e_wm * 64 + ((i + 2 * e_wn) & 3) * 16 + (e_lane & 15);
          f32x4 v = acc[i][j];
          if (sec == 0) {
            u32x2 o = {pack_bf2(v[0] * qscale, v[1] * qscale), pack_bf2(v[2] * qscale, v[3] * qscale)};
            *(u32x2*)(Qg + ((size_t)(head * SEQ + tok) * 32 + d)) = o;
          } else {
            char* kb = (char*)Kf + (size_t)(head * 64 + (tok >> 5)) * 1536;
            const int r = tok & 31;
            unsigned p0 = pack_bf2(v[0], v[1]), p1 = pack_bf2(v[2], v[3]);
            if (d4 < 4) {
              u32x2 o = {p0, p1};
              *(u32x2*)(kb + ((d4 >> 1) * 32 + r) * 16 + (d4 & 1) * 8) = o;
            } else {
              u32x4 o = {p0, p1, 0x00003F80u, 0u};
              *(u32x4*)(kb + 1024 + r * 16) = o;
            }
          }
        }
      } else {
        const int rr = nb + (e_lane & 15) - 2 * DM, head = rr / DH, d = rr - head * DH;
        const int g = e_lane >> 4;
#pragma unroll
        for (int i = 0; i < 4; ++i) {
          f32x4 v = acc[i][j];
          u32x2 w = {pack_bf2(v[0], v[1]), pack_bf2(v[2], v[3])};
          *(u32x2*)(wtr + (e_lane & 15) * 32 + g * 8) = w;
          s16x4 tv = tr_read(wtr + g * 128 + ((e_lane & 15) >> 2) * 32 + (e_lane & 3) * 8);
          const int s = (m0 + e_wm * 64 + ((i + 2 * e_wn) & 3) * 16) >> 4;
          u16* vb = Vf + ((size_t)(head * 128 + s) * 48 + (g & 1) * 24 + d) * 8 + (g >> 1) * 4;
          *(s16x4*)vb = tv;
          if (d == DH - 1) { s16x4 one = {0x3F80, 0x3F80, 0x3F80, 0x3F80}; *(s16x4*)(vb + 8) = one; }
        }
      }
    }
  } else {
#pragma unroll
    for (int j = 0; j < 3; ++j) {
      const int f = (nblk - 10) * 96 + (e_wn * 3 + j) * 16 + ln4;
#pragma unroll
      for (int i = 0; i < 4; ++i) {
        const int tok = m0 + e_wm * 64 + ((i + 2 * e_wn) & 3) * 16 + (e_lane & 15);
        float hv[4];
#pragma unroll
        for (int r = 0; r < 4; ++r) {
          const float g = (e_wm >= 2) ? acc[i][j + 3][r] : acc[i][j][r], u = (e_wm >= 2) ? acc[i][j][r] : acc[i][j + 3][r];
          const float e = __builtin_amdgcn_exp2f(g * __builtin_fmaf(g * g, -0.10294324f, -2.30220819f));
          hv[r] = (g * u) * __builtin_amdgcn_rcpf(1.f + e);
        }
        u32x2 o = {pack_bf2(hv[0], hv[1]), pack_bf2(hv[2], hv[3])};
        *(u32x2*)(hbuf + ((size_t)((tok >> 4) * 54 + (f >> 5)) * 64 + ((f >> 3) & 3) * 16 + (tok & 15)) * 8 + (f & 7)) = o;
      }
    }
  }
}

constexpr int AT_STAGE = 24576;
constexpr float AT_BIAS = 8.f;
#define MFMA32(a, b, c) __builtin_amdgcn_mfma_f32_32x32x16_bf16((a), (b), (c), 0, 0, 0)
#define MFMA16(a, b, c) __builtin_amdgcn_mfma_f32_16x16x32_bf16((a), (b), (c), 0, 0, 0)

DI void attn_role(char* smem, int b, const u16* __restrict__ Qg, const u16* __restrict__ Kf,
                  const u16* __restrict__ Vf, u16* __restrict__ obuf) {
  const int head = (b & 7) * 4 + ((b >> 3) & 3), qb = b >> 5;
  const int tid = threadIdx.x, wave = tid >> 6, lane = tid & 63;
  const int r = lane & 31, h = lane >> 5;
  const int q = qb * 256 + wave * 32 + r;

  const unsigned lds0 = __builtin_amdgcn_readfirstlane((unsigned)(size_t)smem);
  const unsigned wv = __builtin_amdgcn_readfirstlane(wave);
  const char* gsrc = ((wv < 4) ? (const char*)Kf + wv * 3072 : (const char*)Vf + (wv - 4) * 3072) +
                     (size_t)head * 98304 + lane * 16;
  auto stage = [&](unsigned boff, int tile) {
#pragma unroll
    for (int i = 0; i < 3; ++i) glds16(gsrc + tile * 12288 + i * 1024, lds0 + boff + (wv * 3 + i) * 1024);
  };
  const u16* qrow = Qg + (size_t)(head * SEQ + q) * 32;
  bf16x8 q1 = *(const bf16x8*)(qrow + 8 * h);
  u32x4 q2 = {0u, 0u, 0u, 0u};
  {
    u32x2 t2 = *(const u32x2*)(qrow + 16);
    if (h == 0) { q2[0] = t2[0]; q2[1] = t2[1]; }
  }
  stage(0, 0);
  stage(AT_STAGE, 1);

  const int k1_off = lane * 16, k2_off = 1024 + r * 16;
  const int v_off = 12288 + (h * 24 + (r < 23 ? r : 23)) * 16;

  f32x16 o, s0, s1;
  const f32x16 zero = {0.f, 0.f, 0.f, 0.f, 0.f, 0.f, 0.f, 0.f, 0.f, 0.f, 0.f, 0.f, 0.f, 0.f, 0.f, 0.f};
  o = zero;
  float mref;

  asm volatile("s_waitcnt vmcnt(3)" ::: "memory");
  barrier_raw();
  {
    s0 = MFMA32(*(const bf16x8*)(smem + k1_off), q1, zero);
    s0 = MFMA32(*(const bf16x8*)(smem + k2_off), __builtin_bit_cast(bf16x8, q2), s0);
    s1 = MFMA32(*(const bf16x8*)(smem + 1536 + k1_off), q1, zero);
    s1 = MFMA32(*(const bf16x8*)(smem + 1536 + k2_off), __builtin_bit_cast(bf16x8, q2), s1);
    float mx = fmaxf(s0[0], s1[0]);
#pragma unroll
    for (int i = 1; i < 16; ++i) mx = fmaxf(mx, fmaxf(s0[i], s1[i]));
    mref = rbf(fmaxf(mx, __shfl_xor(mx, 32)) + AT_BIAS);
    if (h == 0) q2[2] = pack_bf2(-mref, 0.f);
#pragma unroll
    for (int i = 0; i < 16; ++i) { s0[i] -= mref; s1[i] -= mref; }
  }
  unsigned cur = 0;
  for (int tile = 0; tile < 8; ++tile) {
    const unsigned nxt = (cur == 2 * AT_STAGE) ? 0 : cur + AT_STAGE;
#pragma unroll
    for (int g = 0; g < 4; ++g) {
      f32x16 n0, n1;
      const char* kb;
      if (g == 3) {
        if (tile < 7) {
          wait_vm0();
          barrier_raw();
          if (tile < 6) stage((nxt == 2 * AT_STAGE) ? 0 : nxt + AT_STAGE, tile + 2);
        }
        kb = smem + nxt;
      } else {
        kb = smem + cur + (g + 1) * 3072;
      }
      n0 = MFMA32(*(const bf16x8*)(kb + k1_off), q1, zero);
      n0 = MFMA32(*(const bf16x8*)(kb + k2_off), __builtin_bit_cast(bf16x8, q2), n0);
      n1 = MFMA32(*(const bf16x8*)(kb + 1536 + k1_off), q1, zero);
      n1 = MFMA32(*(const bf16x8*)(kb + 1536 + k2_off), __builtin_bit_cast(bf16x8, q2), n1);
      unsigned p[16];
#pragma unroll
      for (int i = 0; i < 8; ++i) {
        p[i] = pack_bf2(__builtin_amdgcn_exp2f(s0[2 * i]), __builtin_amdgcn_exp2f(s0[2 * i + 1]));
        p[8 + i] = pack_bf2(__builtin_amdgcn_exp2f(s1[2 * i]), __builtin_amdgcn_exp2f(s1[2 * i + 1]));
      }
      unsigned orr = p[0];
#pragma unroll
      for (int i = 1; i < 16; ++i) orr |= p[i];
      if (__builtin_amdgcn_ballot_w64((orr & 0x40004000u) != 0) != 0) {
        const char* kc = smem + cur + g * 3072;
        float mx = -3.0e38f;
#pragma unroll
        for (int hf = 0; hf < 2; ++hf) {
          f32x16 t = MFMA32(*(const bf16x8*)(kc + hf * 1536 + k1_off), q1, zero);
          t = MFMA32(*(const bf16x8*)(kc + hf * 1536 + k2_off), __builtin_bit_cast(bf16x8, q2), t);
#pragma unroll
          for (int i = 0; i < 16; ++i) mx = fmaxf(mx, t[i]);
          __builtin_amdgcn_sched_barrier(0);
        }
        mx = fmaxf(mx, __shfl_xor(mx, 32));
        const float mnew = rbf(mref + fmaxf(mx + AT_BIAS, 0.f));
        const float delta = mnew - mref;
        const float alpha = __builtin_amdgcn_exp2f(-delta);
        mref = mnew;
        if (h == 0) q2[2] = pack_bf2(-mref, 0.f);
#pragma unroll
        for (int i = 0; i < 16; ++i) { n0[i] -= delta; n1[i] -= delta; o[i] *= alpha; }
#pragma unroll
        for (int hf = 0; hf < 2; ++hf) {
          __builtin_amdgcn_sched_barrier(0);
          f32x16 t = MFMA32(*(const bf16x8*)(kc + hf * 1536 + k1_off), q1, zero);
          t = MFMA32(*(const bf16x8*)(kc + hf * 1536 + k2_off), __builtin_bit_cast(bf16x8, q2), t);
#pragma unroll
          for (int i = 0; i < 8; ++i)
            p[hf * 8 + i] = pack_bf2(__builtin_amdgcn_exp2f(t[2 * i]), __builtin_amdgcn_exp2f(t[2 * i + 1]));
        }
      }
#pragma unroll
      for (int s = 0; s < 4; ++s) {
        u32x4 pv = {p[4 * s], p[4 * s + 1], p[4 * s + 2], p[4 * s + 3]};
        bf16x8 vf = *(const bf16x8*)(smem + cur + (g * 4 + s) * 768 + v_off);
        o = MFMA32(vf, __builtin_bit_cast(bf16x8, pv), o);
      }
      s0 = n0; s1 = n1;
    }
    cur = nxt;
  }
  const float l = __shfl(o[8], r + 32);
  const float inv = 1.f / l;
  auto ostore = [&](int d, u32x2 w) {
    const int col = head * DH + d;
    *(u32x2*)(obuf + ((size_t)((q >> 4) * 20 + (col >> 5)) * 64 + ((col >> 3) & 3) * 16 + (q & 15)) * 8 + (col & 7)) = w;
  };
  {
    u32x2 w0 = {pack_bf2(o[0] * inv, o[1] * inv), pack_bf2(o[2] * inv, o[3] * inv)};
    u32x2 w1 = {pack_bf2(o[4] * inv, o[5] * inv), pack_bf2(o[6] * inv, o[7] * inv)};
    ostore(4 * h, w0);
    ostore(4 * h + 8, w1);
    if (h == 0) {
      u32x2 w2 = {pack_bf2(o[8] * inv, o[9] * inv), pack_bf2(o[10] * inv, o[11] * inv)};
      ostore(16, w2);
    }
  }
}

DI void down_role(char* smem, int b, const u16* __restrict__ hbuf, const u16* __restrict__ Bf,
                  const u16* __restrict__ x, float* __restrict__ y) {
  const int t = (b & 7) * 32 + (b >> 3);
  const int nblk = t & 7, mblk = t >> 3;
  const int tid = threadIdx.x, wave = tid >> 6, lane = tid & 63;
  const int m0 = mblk * 64, n0 = nblk * 80;
  const unsigned wv = __builtin_amdgcn_readfirstlane(wave);
  const int mh = wave & 1, ks = wave >> 1;
  const u16* ap = hbuf + ((size_t)((mblk * 4 + mh * 2) * 54 + ks) * 64 + lane) * 8;
  const u16* bp = Bf + ((size_t)((nblk * 54 + ks) * 5) * 64 + lane) * 8;
  f32x4 acc[2][5];
#pragma unroll
  for (int i = 0; i < 2; ++i)
#pragma unroll
    for (int j = 0; j < 5; ++j) acc[i][j] = (f32x4){0.f, 0.f, 0.f, 0.f};
  bf16x8 a0[2], b0[5], a1[2], b1[5];
  auto load = [&](bf16x8 (&af)[2], bf16x8 (&bfr)[5], int it) {
    af[0] = *(const bf16x8*)(ap + it * 4 * 512);
    af[1] = *(const bf16x8*)(ap + 54 * 512 + it * 4 * 512);
#pragma unroll
    for (int j = 0; j < 5; ++j) bfr[j] = *(const bf16x8*)(bp + (it * 20 + j) * 512);
  };
  auto mma = [&](bf16x8 (&af)[2], bf16x8 (&bfr)[5]) {
#pragma unroll
    for (int j = 0; j < 5; ++j) {
      acc[0][j] = MFMA16(bfr[j], af[0], acc[0][j]);
      acc[1][j] = MFMA16(bfr[j], af[1], acc[1][j]);
    }
  };
  load(a0, b0, 0);
  for (int it = 0; it < 12; it += 2) {
    load(a1, b1, it + 1);
    mma(a0, b0);
    load(a0, b0, it + 2);
    mma(a1, b1);
  }
  if (wv < 4) load(a1, b1, 13);
  mma(a0, b0);
  if (wv < 4) mma(a1, b1);
  if (wv >= 2) {
    f32x4* red = (f32x4*)smem + (wave - 2) * 640 + lane;
#pragma unroll
    for (int i = 0; i < 2; ++i)
#pragma unroll
      for (int j = 0; j < 5; ++j) red[(i * 5 + j) * 64] = acc[i][j];
  }
  barrier_raw();
  if (wv < 2) {
#pragma unroll
    for (int i = 0; i < 2; ++i) {
      const size_t gi = (size_t)(m0 + mh * 32 + i * 16 + (lane & 15)) * DM + n0 + (lane >> 4) * 4;
#pragma unroll
      for (int j = 0; j < 5; ++j) {
        f32x4 s = acc[i][j];
#pragma unroll
        for (int w = 0; w < 3; ++w) s += ((const f32x4*)smem)[(w * 2 + mh) * 640 + (i * 5 + j) * 64 + lane];
        u32x2 xv = *(const u32x2*)(x + gi + j * 16);
        s[0] += bf_lo(xv[0]); s[1] += bf_hi(xv[0]); s[2] += bf_lo(xv[1]); s[3] += bf_hi(xv[1]);
        *(f32x4*)(y + ((size_t)(t * 20 + mh * 10 + i * 5 + j) * 64 + lane) * 4) = s;
      }
    }
  }
}

__global__ __launch_bounds__(512, 4) void k2_kernel(const u16* __restrict__ Qg, const u16* __restrict__ Kf,
                                                    const u16* __restrict__ Vf, u16* __restrict__ obuf,
                                                    const u16* __restrict__ hbuf, const u16* __restrict__ Bf,
                                                    const u16* __restrict__ x, float* __restrict__ y) {
  __shared__ __attribute__((aligned(16))) char smem[3 * AT_STAGE];
  const int b = blockIdx.x;
  if (b < 256) attn_role(smem, b, Qg, Kf, Vf, obuf);
  else down_role(smem, b - 256, hbuf, Bf, x, y);
}

__global__ __launch_bounds__(512, 2) void k3_kernel(const u16* __restrict__ obuf, const u16* __restrict__ Bt,
                                                    const float* __restrict__ y, u16* __restrict__ out) {
  __shared__ __attribute__((aligned(16))) char smem[80 * 1280];
  const int b = blockIdx.x;
  const int t = (b & 7) * 32 + (b >> 3);
  const int nblk = t & 7, mblk = t >> 3;
  const int tid = threadIdx.x, wave = tid >> 6, lane = tid & 63;
  const int m0 = mblk * 64, n0 = nblk * 80;
  const unsigned lds0 = __builtin_amdgcn_readfirstlane((unsigned)(size_t)smem);
  const unsigned wv = __builtin_amdgcn_readfirstlane(wave);
  {
    const unsigned b_vo = nblk * 80 * 1280 + wave * 1024 + lane * 16;
#pragma unroll
    for (int i = 0; i < 13; ++i)
      if (i < 12 || wv < 4) glds16s(Bt + i * 4096, b_vo, lds0 + (i * 8 + wv) * 1024);
  }
  const int mh = wave & 1, ks = wave >> 1;
  bf16x8 af[5][2];
  {
    const u16* ap = obuf + ((size_t)((mblk * 4 + mh * 2) * 20 + ks) * 64 + lane) * 8;
#pragma unroll
    for (int s = 0; s < 5; ++s) {
      af[s][0] = *(const bf16x8*)(ap + s * 4 * 512);
      af[s][1] = *(const bf16x8*)(ap + 20 * 512 + s * 4 * 512);
    }
  }
  f32x4 yv[3];
  {
    const float* yp = y + ((size_t)(t * 20 + wave) * 64 + lane) * 4;
    yv[0] = *(const f32x4*)(yp);
    yv[1] = *(const f32x4*)(yp + 8 * 256);
    yv[2] = *(const f32x4*)(yp + (wv < 4 ? 16 : 8) * 256);
  }
  f32x4 acc[2][5];
#pragma unroll
  for (int i = 0; i < 2; ++i)
#pragma unroll
    for (int j = 0; j < 5; ++j) acc[i][j] = (f32x4){0.f, 0.f, 0.f, 0.f};
  int b_off;
  {
    int g = lane >> 4, q = (lane & 15) >> 2, p = lane & 3;
    b_off = (ks * 4 + g) * 1280 + ((g & 1) << 7) + q * 32 + p * 8;
  }
  wait_vm0();
  barrier_raw();
#pragma unroll
  for (int s = 0; s < 5; ++s) {
    bf16x8 bfr[5];
#pragma unroll
    for (int j = 0; j < 5; ++j) {
      s16x4 lo = tr_read(smem + b_off + s * 20480 + j * 256);
      s16x4 hi = tr_read(smem + (b_off ^ 128) + s * 20480 + j * 256);
      bfr[j] = __builtin_shufflevector(lo, hi, 0, 1, 2, 3, 4, 5, 6, 7);
    }
#pragma unroll
    for (int j = 0; j < 5; ++j) {
      acc[0][j] = MFMA16(bfr[j], af[s][0], acc[0][j]);
      acc[1][j] = MFMA16(bfr[j], af[s][1], acc[1][j]);
    }
  }
  barrier_raw();
  {
    f32x4* red = (f32x4*)smem + (ks * 20 + mh * 10) * 64 + lane;
#pragma unroll
    for (int i = 0; i < 2; ++i)
#pragma unroll
      for (int j = 0; j < 5; ++j) red[(i * 5 + j) * 64] = acc[i][j];
  }
  barrier_raw();
#pragma unroll
  for (int u = 0; u < 3; ++u) {
    const int tile = wave + u * 8;
    if (u < 2 || wv < 4) {
      f32x4 s = yv[u];
#pragma unroll
      for (int w = 0; w < 4; ++w) s += ((const f32x4*)smem)[(w * 20 + tile) * 64 + lane];
      const int rt = tile / 5, j = tile - rt * 5;
      u32x2 o = {pack_bf2(s[0], s[1]), pack_bf2(s[2], s[3])};
      *(u32x2*)(out + (size_t)(m0 + rt * 16 + (lane & 15)) * DM + n0 + j * 16 + (lane >> 4) * 4) = o;
    }
  }
}

extern "C" void kernel_launch(void* const* d_in, const int* in_sizes, int n_in, void* d_out, int out_size,
                              void* d_ws, size_t ws_size, hipStream_t stream) {
  const u16* x = (const u16*)d_in[0];
  const u16* wqkv = (const u16*)d_in[1];
  const u16* wo = (const u16*)d_in[2];
  const u16* wg = (const u16*)d_in[4];
  const u16* wu = (const u16*)d_in[5];
  const u16* wd = (const u16*)d_in[6];
  char* ws = (char*)d_ws;
  u16* Qg = (u16*)(ws + (4u << 20));
  u16* Kf = (u16*)(ws + (8u << 20));
  u16* Vf = (u16*)(ws + (12u << 20));
  u16* hb = (u16*)(ws + (16u << 20));
  u16* ob = (u16*)(ws + (24u << 20));
  float* yb = (float*)(ws + (28u << 20));
  u16* Bt = (u16*)(ws + (34u << 20));
  u16* Bf = (u16*)(ws + (35u << 20));
  gemm1_kernel<<<256, 512, 0, stream>>>(x, wqkv, wg, wu, Qg, Kf, Vf, hb, wd, wo, Bt, Bf);
  k2_kernel<<<512, 512, 0, stream>>>(Qg, Kf, Vf, ob, hb, Bf, x, yb);
  k3_kernel<<<256, 512, 0, stream>>>(ob, Bt, yb, (u16*)d_out);
}
```

```cpp
#include <hip/hip_runtime.h>
#include <stdint.h>

typedef __attribute__((ext_vector_type(8))) short bf16x8;
typedef __attribute__((ext_vector_type(4))) short s16x4;
typedef __attribute__((ext_vector_type(4))) float f32x4;
typedef __attribute__((ext_vector_type(2))) float f32x2;
typedef __attribute__((ext_vector_type(16))) float f32x16;
typedef __attribute__((ext_vector_type(2))) __bf16 bf2_t;
typedef __attribute__((ext_vector_type(8))) __bf16 bf8_t;
typedef __attribute__((ext_vector_type(4))) unsigned u32x4;
typedef __attribute__((ext_vector_type(2))) unsigned u32x2;
typedef unsigned short u16;
typedef __attribute__((address_space(3))) s16x4 lds_s16x4;

#define DI __device__ __forceinline__

constexpr int SEQ = 2048, DM = 640, NH = 32, DH = 20, DF = 1728;

DI unsigned pack_bf2(float a, float b) {
  f32x2 v = {a, b};
  return __builtin_bit_cast(unsigned, __builtin_convertvector(v, bf2_t));
}
DI float bf_lo(unsigned u) { return __uint_as_float(u << 16); }
DI float bf_hi(unsigned u) { return __uint_as_float(u & 0xffff0000u); }
DI float rbf(float x) { return bf_lo(pack_bf2(x, 0.f)); }

DI void glds16(const void* g, unsigned lds_addr) {
  unsigned keep;
  asm volatile("s_mov_b32 %0, m0\n\ts_mov_b32 m0, %2\n\ts_nop 0\n\tglobal_load_lds_dwordx4 %1, off\n\ts_mov_b32 m0, %0"
               : "=&s"(keep) : "v"(g), "s"(lds_addr) : "memory");
}
DI void glds16s(const void* sbase, unsigned voff, unsigned lds_addr) {
  unsigned keep;
  asm volatile("s_mov_b32 %0, m0\n\ts_mov_b32 m0, %3\n\ts_nop 0\n\tglobal_load_lds_dwordx4 %1, %2\n\ts_mov_b32 m0, %0"
               : "=&s"(keep) : "v"(voff), "s"(sbase), "s"(lds_addr) : "memory");
}
DI void wait_vm0() { asm volatile("s_waitcnt vmcnt(0)" ::: "memory"); }
DI void barrier_raw() {
  asm volatile("s_waitcnt lgkmcnt(0)" ::: "memory");
  __builtin_amdgcn_s_barrier();
  asm volatile("" ::: "memory");
}
template <int V> struct IC { static constexpr int value = V; };
template <int N, int I = 0, class F> DI void static_for(F&& f) {
  if constexpr (I < N) { f(IC<I>{}); static_for<N, I + 1>(f); }
}
DI s16x4 tr_read(const char* lds) {
  return __builtin_amdgcn_ds_read_tr16_b64_v4i16((lds_s16x4*)lds);
}

constexpr int G1_BOFF = 3 * 32768;
__global__ __launch_bounds__(512, 2) void gemm1_kernel(const u16* __restrict__ x, const u16* __restrict__ wqkv,
                                                       const u16* __restrict__ wg, const u16* __restrict__ wu,
                                                       u16* __restrict__ Qg, u16* __restrict__ Kf,
                                                       u16* __restrict__ Vf, u16* __restrict__ hbuf,
                                                       const u16* __restrict__ wd, const u16* __restrict__ wo,
                                                       u16* __restrict__ Bt, u16* __restrict__ Bf) {
  __shared__ __attribute__((aligned(16))) char smem[G1_BOFF + 2 * 24576];
  const int b = blockIdx.x;
  if (b >= 224) {
    const int tsp = (b - 224) * 512 + threadIdx.x;
    {
      u32x4 v[4];
#pragma unroll
      for (int u = 0; u < 4; ++u) {
        int it = tsp + u * 16384;
        if (it >= 640 * 80) it = 0;
        const int k = it / 80, c8 = it - k * 80;
        v[u] = *(const u32x4*)(wo + (size_t)k * DM + c8 * 8);
      }
#pragma unroll
      for (int u = 0; u < 4; ++u) {
        const int it = tsp + u * 16384;
        const int k = it / 80, c8 = it - k * 80;
        const int nb = c8 / 10, r10 = c8 - nb * 10, kb = k >> 3;
        u16* dst = Bt + ((size_t)(nb * 80 + kb) * 640 + (r10 >> 1) * 128 + ((k & 7) ^ ((kb & 1) << 2)) * 16 + (r10 & 1) * 8);
        if (it < 640 * 80) *(u32x4*)dst = v[u];
      }
    }
    for (int it = tsp; it < 216 * 640; it += 16384) {
      const int kb = it / 640, n = it - kb * 640;
      const u16* src = wd + (size_t)kb * 8 * DM + n;
      unsigned e[8];
#pragma unroll
      for (int j = 0; j < 8; ++j) e[j] = src[j * DM];
      const int nb = n / 80, r = n - nb * 80;
      u32x4 w = {e[0] | (e[1] << 16), e[2] | (e[3] << 16), e[4] | (e[5] << 16), e[6] | (e[7] << 16)};
      *(u32x4*)(Bf + ((size_t)((nb * 54 + (kb >> 2)) * 5 + (r >> 4)) * 64 + (kb & 3) * 16 + (r & 15)) * 8) = w;
    }
    return;
  }
  const int t = (b & 7) * 28 + (b >> 3);
  const int nblk = t >> 3, mblk = t & 7;
  const int tid = threadIdx.x, wave = tid >> 6, lane = tid & 63;
  const int wm = wave >> 1, wn = wave & 1;
  const int m0 = mblk * 256;
  const bool isqkv = nblk < 10;

  const unsigned a_vo = ((m0 + wave * 32 + (lane >> 3)) * DM + ((lane & 7) ^ (lane >> 4)) * 8) * 2;
  const int ldb = isqkv ? 3 * DM : DF;
  const u16* b_src[3];
#pragma unroll
  for (int i = 0; i < 3; ++i) {
    int kb = wave;
    int nt = i * 4 + (lane >> 4);
    int rr = ((lane >> 1) & 7) ^ ((kb & 1) << 2);
    int k = kb * 8 + rr;
    const u16* base;
    if (isqkv) base = wqkv + nblk * 192 + nt * 16;
    else base = (nt < 6) ? (wg + (nblk - 10) * 96 + nt * 16) : (wu + (nblk - 10) * 96 + (nt - 6) * 16);
    b_src[i] = base + (size_t)k * ldb + (lane & 1) * 8;
  }
  const unsigned lds0 = __builtin_amdgcn_readfirstlane((unsigned)(size_t)smem);
  const unsigned wv = __builtin_amdgcn_readfirstlane(wave);
  auto stage_a = [&](int kt, int g) {
    glds16s(x + g * 8 * DM + kt * 64, a_vo ^ ((g & 1) << 6), lds0 + (kt % 3) * 32768 + (wv * 4 + g) * 1024);
  };
  auto stage_b = [&](int kt, int g) {
    glds16(b_src[g] + (size_t)kt * 64 * ldb, lds0 + G1_BOFF + (kt & 1) * 24576 + (wv * 3 + g) * 1024);
  };
#pragma unroll
  for (int kt = 0; kt < 2; ++kt) {
#pragma unroll
    for (int g = 0; g < 4; ++g) stage_a(kt, g);
#pragma unroll
    for (int g = 0; g < 3; ++g) stage_b(kt, g);
  }

  const int a_off = ((wm * 64 + wn * 32 + (lane & 15)) * 128 + (((lane >> 4) ^ ((lane >> 1) & 7)) << 4)) ^ ((wm & 1) << 6);
  int b_off[2][2];
  {
    int g = lane >> 4, q = (lane & 15) >> 2, p = lane & 3;
    int base = G1_BOFF + g * 3072 + ((g & 1) << 7) + q * 32 + p * 8;
    int t0 = (isqkv ? wn * 6 : wn * 3) * 256, t1 = (isqkv ? wn * 6 + 3 : 6 + wn * 3) * 256;
    if (wm >= 2) { int tt = t0; t0 = t1; t1 = tt; }
#pragma unroll
    for (int kk = 0; kk < 2; ++kk) {
      b_off[kk][0] = base + t0 + (kk ^ (wm & 1)) * 12288;
      b_off[kk][1] = base + t1 + (kk ^ (wm & 1)) * 12288;
    }
  }

  f32x4 acc[4][6];
#pragma unroll
  for (int i = 0; i < 4; ++i)
#pragma unroll
    for (int j = 0; j < 6; ++j) acc[i][j] = (f32x4){0.f, 0.f, 0.f, 0.f};

  bf16x8 af0[4], bf0[6], af1[4], bf1[6];
  auto read_q = [&](bf16x8 (&af)[4], bf16x8 (&bfr)[6], int kt, int kk, int g) {
    const char* sa = smem + (kt % 3) * 32768;
    const char* sb = smem + (kt & 1) * 24576;
    auto rb = [&](int j) {
      s16x4 lo = tr_read(sb + b_off[kk][j / 3] + (j % 3) * 256);
      s16x4 hi = tr_read(sb + (b_off[kk][j / 3] ^ 128) + (j % 3) * 256);
      bfr[j] = __builtin_shufflevector(lo, hi, 0, 1, 2, 3, 4, 5, 6, 7);
    };
    auto ra = [&](int i) { af[i] = *(const bf16x8*)(sa + ((a_off ^ (kk << 6)) ^ ((i >> 1) << 12)) + (i & 1) * 2048); };
    if (g == 0) { ra(0); rb(0); rb(1); }
    if (g == 1) { rb(2); rb(3); }
    if (g == 2) { rb(4); rb(5); }
    if (g == 3) { ra(1); ra(2); ra(3); }
  };
  auto mma_q = [&](bf16x8 (&af)[4], bf16x8 (&bfr)[6], int i) {
#pragma unroll
    for (int j = 0; j < 6; ++j)
      acc[i][j] = __builtin_amdgcn_mfma_f32_16x16x32_bf16(bfr[j], af[i], acc[i][j], 0, 0, 0);
  };

  float rs[2] = {0.f, 0.f}, rq[2] = {0.f, 0.f};
  f32x4 cs[3];
#pragma unroll
  for (int j = 0; j < 3; ++j) cs[j] = (f32x4){0.f, 0.f, 0.f, 0.f};
  const bf16x8 ones = {0x3F80, 0x3F80, 0x3F80, 0x3F80, 0x3F80, 0x3F80, 0x3F80, 0x3F80};
  auto stats_q = [&](bf16x8 (&af)[4], bf16x8 (&bfr)[6], int kk, int g) {
    if (g < 2) {
      const bf8_t w = __builtin_bit_cast(bf8_t, af[g]);
      const bf2_t v[4] = {__builtin_shufflevector(w, w, 0, 1), __builtin_shufflevector(w, w, 2, 3),
                          __builtin_shufflevector(w, w, 4, 5), __builtin_shufflevector(w, w, 6, 7)};
#pragma unroll
      for (int c = 0; c < 4; ++c) {
        rs[g] = __builtin_amdgcn_fdot2_f32_bf16(v[c], __builtin_bit_cast(bf2_t, 0x3F803F80u), rs[g], false);
        rq[g] = __builtin_amdgcn_fdot2_f32_bf16(v[c], v[c], rq[g], false);
      }
    }
    if (g < 3 && kk == 0) cs[g] = __builtin_amdgcn_mfma_f32_16x16x32_bf16(bfr[g], ones, cs[g], 0, 0, 0);
  };

  asm volatile("s_waitcnt vmcnt(7)" ::: "memory");
  barrier_raw();
#pragma unroll
  for (int g = 0; g < 4; ++g) read_q(af0, bf0, 0, 0, g);
#pragma unroll
  for (int kt = 0; kt < 10; ++kt) {
#pragma unroll
    for (int g = 0; g < 4; ++g) {
      __builtin_amdgcn_sched_barrier(0);
      read_q(af1, bf1, kt, 1, g);
      mma_q(af0, bf0, g);
      __builtin_amdgcn_sched_barrier(0);
      stats_q(af0, bf0, 0, g);
      if (g < 2) asm volatile("" : "+v"(rs[g & 1]), "+v"(rq[g & 1]));
      if (kt + 2 < 10) stage_a(kt + 2, g);
    }
    __builtin_amdgcn_sched_barrier(0);
    if (kt + 1 < 10) {
      if (kt + 2 < 10) asm volatile("s_waitcnt vmcnt(4)" ::: "memory");
      else wait_vm0();
      barrier_raw();
    }
#pragma unroll
    for (int g = 0; g < 4; ++g) {
      __builtin_amdgcn_sched_barrier(0);
      if (kt + 1 < 10) read_q(af0, bf0, kt + 1, 0, g);
      mma_q(af1, bf1, g);
      __builtin_amdgcn_sched_barrier(0);
      stats_q(af1, bf1, 1, g);
      if (g < 2) asm volatile("" : "+v"(rs[g & 1]), "+v"(rq[g & 1]));
      if (kt + 2 < 10 && g < 3) stage_b(kt + 2, g);
    }
  }
  __builtin_amdgcn_sched_barrier(0);
  barrier_raw();

  int tz = threadIdx.x;
  asm volatile("" : "+v"(tz));
  const int e_wave = tz >> 6, e_lane = tz & 63, e_wm = e_wave >> 1, e_wn = e_wave & 1;
  const int ln4 = (e_lane >> 4) * 4;
  {
    f32x2* st_row = (f32x2*)(smem + 65536);
    float* st_col = (float*)(smem + 65536 + 4096);
#pragma unroll
    for (int i = 0; i < 2; ++i) {
      float a = rs[i], q = rq[i];
      a += __shfl_xor(a, 16); q += __shfl_xor(q, 16);
      a += __shfl_xor(a, 32); q += __shfl_xor(q, 32);
      if (e_lane < 16) st_row[e_wm * 64 + e_wn * 32 + i * 16 + e_lane] = (f32x2){a, q};
    }
    if ((e_lane & 15) == 0) {
#pragma unroll
      for (int j = 0; j < 3; ++j)
        *(f32x4*)(st_col + ((e_wm & 1) * 2 + e_wn) * 96 + ((e_wm >> 1) * 3 + j) * 16 + ln4) = cs[j];
    }
    barrier_raw();
    float mb[4], inv[4];
#pragma unroll
    for (int i = 0; i < 4; ++i) {
      const f32x2 p0 = st_row[e_wm * 64 + ((i + 2 * e_wn) & 3) * 16 + (e_lane & 15)];
      const float mean = p0[0] * (1.f / DM);
      const float var = fmaxf(p0[1] * (1.f / DM) - mean * mean, 0.f);
      mb[i] = rbf(mean);
      inv[i] = 1.f / rbf(sqrtf(rbf(rbf(var) + 1e-5f)));
    }
#pragma unroll
    for (int j = 0; j < 6; ++j) {
      const int jl = (e_wm >= 2) ? (j + 3) % 6 : j;
      const f32x4 c = *(const f32x4*)(st_col + e_wn * 96 + jl * 16 + ln4) + *(const f32x4*)(st_col + (2 + e_wn) * 96 + jl * 16 + ln4);
#pragma unroll
      for (int i = 0; i < 4; ++i)
#pragma unroll
        for (int r = 0; r < 4; ++r) acc[i][j][r] = __builtin_fmaf(-mb[i], c[r], acc[i][j][r]) * inv[i];
    }
  }
  if (isqkv) {
    const float qscale = 0.22360679774997896f * 1.4426950408889634f;
    char* wtr = smem + e_wave * 512;
#pragma unroll
    for (int j = 0; j < 6; ++j) {
      const int nb = nblk * 192 + (e_wn * 6 + ((e_wm >= 2) ? (j + 3) % 6 : j)) * 16;
      const int sec = nb / DM;
      if (sec < 2) {
        const int rr = nb + ln4 - sec * DM, head = rr / DH, d = rr - head * DH, d4 = d >> 2;
#pragma unroll
        for (int i = 0; i < 4; ++i) {
          const int tok = m0 + e_wm * 64 + ((i + 2 * e_wn) & 3) * 16 + (e_lane & 15);
          f32x4 v = acc[i][j];
          if (sec == 0) {
            u32x2 o = {pack_bf2(v[0] * qscale, v[1] * qscale), pack_bf2(v[2] * qscale, v[3] * qscale)};
            *(u32x2*)(Qg + ((size_t)((tok >> 4) * 40 + (nb >> 4)) * 64 + e_lane) * 4) = o;
          } else {
            char* kb = (char*)Kf + (size_t)(head * 64 + (tok >> 5)) * 1536;
            const int r = tok & 31;
            unsigned p0 = pack_bf2(v[0], v[1]), p1 = pack_bf2(v[2], v[3]);
            if (d4 < 4) {
              u32x2 o = {p0, p1};
              *(u32x2*)(kb + ((d4 >> 1) * 32 + r) * 16 + (d4 & 1) * 8) = o;
            } else {
              u32x4 o = {p0, p1, 0x00003F80u, 0u};
              *(u32x4*)(kb + 1024 + r * 16) = o;
            }
          }
        }
      } else {
        const int rr = nb + (e_lane & 15) - 2 * DM, head = rr / DH, d = rr - head * DH;
        const int g = e_lane >> 4;
#pragma unroll
        for (int i = 0; i < 4; ++i) {
          f32x4 v = acc[i][j];
          u32x2 w = {pack_bf2(v[0], v[1]), pack_bf2(v[2], v[3])};
          *(u32x2*)(wtr + (e_lane & 15) * 32 + g * 8) = w;
          s16x4 tv = tr_read(wtr + g * 128 + ((e_lane & 15) >> 2) * 32 + (e_lane & 3) * 8);
          const int s = (m0 + e_wm * 64 + ((i + 2 * e_wn) & 3) * 16) >> 4;
          u16* vb = Vf + ((size_t)(head * 128 + s) * 48 + (g & 1) * 24 + d) * 8 + (g >> 1) * 4;
          *(s16x4*)vb = tv;
          if (d == DH - 1) { s16x4 one = {0x3F80, 0x3F80, 0x3F80, 0x3F80}; *(s16x4*)(vb + 8) = one; }
        }
      }
    }
  } else {
#pragma unroll
    for (int j = 0; j < 3; ++j) {
      const int f = (nblk - 10) * 96 + (e_wn * 3 + j) * 16 + ln4;
#pragma unroll
      for (int i = 0; i < 4; ++i) {
        const int tok = m0 + e_wm * 64 + ((i + 2 * e_wn) & 3) * 16 + (e_lane & 15);
        float hv[4];
#pragma unroll
        for (int r = 0; r < 4; ++r) {
          const float g = (e_wm >= 2) ? acc[i][j + 3][r] : acc[i][j][r], u = (e_wm >= 2) ? acc[i][j][r] : acc[i][j + 3][r];
          const float e = __builtin_amdgcn_exp2f(g * __builtin_fmaf(g * g, -0.10294324f, -2.30220819f));
          hv[r] = (g * u) * __builtin_amdgcn_rcpf(1.f + e);
        }
        u32x2 o = {pack_bf2(hv[0], hv[1]), pack_bf2(hv[2], hv[3])};
        *(u32x2*)(hbuf + ((size_t)((tok >> 4) * 54 + (f >> 5)) * 64 + ((f >> 3) & 3) * 16 + (tok & 15)) * 8 + (f & 7)) = o;
      }
    }
  }
}

constexpr int AT_STAGE = 24576;
#define AT_BIAS_V 8.f
constexpr float AT_BIAS = AT_BIAS_V;
#define AT_LIMIT_V 1073741824.f
constexpr float AT_LIMIT = AT_LIMIT_V;
#define MFMA32(a, b, c) __builtin_amdgcn_mfma_f32_32x32x16_bf16((a), (b), (c), 0, 0, 0)
#define MFMA16(a, b, c) __builtin_amdgcn_mfma_f32_16x16x32_bf16((a), (b), (c), 0, 0, 0)

DI void attn_role(char* smem, int b, const u16* __restrict__ Qg, const u16* __restrict__ Kf,
                  const u16* __restrict__ Vf, u16* __restrict__ obuf) {
  const int head = (b & 7) * 4 + ((b >> 3) & 3), qb = b >> 5;
  const int tid = threadIdx.x, wave = tid >> 6, lane = tid & 63;
  const int r = lane & 31, h = lane >> 5;
  const int q = qb * 256 + wave * 32 + r;

  const unsigned lds0 = __builtin_amdgcn_readfirstlane((unsigned)(size_t)smem);
  const unsigned wv = __builtin_amdgcn_readfirstlane(wave);
  const char* gsrc = ((wv < 4) ? (const char*)Kf + wv * 3072 : (const char*)Vf + (wv - 4) * 3072) +
                     (size_t)head * 98304 + lane * 16;
  auto stage = [&](unsigned boff, int tile) {
#pragma unroll
    for (int i = 0; i < 3; ++i) glds16(gsrc + tile * 12288 + i * 1024, lds0 + boff + (wv * 3 + i) * 1024);
  };
  auto qload = [&](int d) {
    const int col = head * DH + d;
    return *(const u32x2*)(Qg + ((size_t)((q >> 4) * 40 + (col >> 4)) * 64 + ((col >> 2) & 3) * 16 + (q & 15)) * 4);
  };
  bf16x8 q1;
  u32x4 q2 = {0u, 0u, 0u, 0u};
  {
    u32x2 ta = qload(8 * h), tb = qload(8 * h + 4), t2 = qload(16);
    u32x4 t = {ta[0], ta[1], tb[0], tb[1]};
    q1 = __builtin_bit_cast(bf16x8, t);
    if (h == 0) { q2[0] = t2[0]; q2[1] = t2[1]; }
  }
  stage(0, 0);
  stage(AT_STAGE, 1);

  const int k1_off = lane * 16, k2_off = 1024 + r * 16;
  const int v_off = 12288 + (h * 24 + (r < 23 ? r : 23)) * 16;

  f32x16 o, s0, s1;
  const f32x16 zero = {0.f, 0.f, 0.f, 0.f, 0.f, 0.f, 0.f, 0.f, 0.f, 0.f, 0.f, 0.f, 0.f, 0.f, 0.f, 0.f};
  o = zero;
  float mref;

  asm volatile("s_waitcnt vmcnt(3)" ::: "memory");
  barrier_raw();
  {
    s0 = MFMA32(*(const bf16x8*)(smem + k1_off), q1, zero);
    s0 = MFMA32(*(const bf16x8*)(smem + k2_off), __builtin_bit_cast(bf16x8, q2), s0);
    s1 = MFMA32(*(const bf16x8*)(smem + 1536 + k1_off), q1, zero);
    s1 = MFMA32(*(const bf16x8*)(smem + 1536 + k2_off), __builtin_bit_cast(bf16x8, q2), s1);
    float mx = fmaxf(s0[0], s1[0]);
#pragma unroll
    for (int i = 1; i < 16; ++i) mx = fmaxf(mx, fmaxf(s0[i], s1[i]));
    mref = rbf(fmaxf(mx, __shfl_xor(mx, 32)) + AT_BIAS);
    if (h == 0) q2[2] = pack_bf2(-mref, 0.f);
#pragma unroll
    for (int i = 0; i < 16; ++i) { s0[i] -= mref; s1[i] -= mref; }
  }
  unsigned cur = 0;
  for (int tile = 0; tile < 8; ++tile) {
    const unsigned nxt = (cur == 2 * AT_STAGE) ? 0 : cur + AT_STAGE;
    float osave[12];
#pragma unroll
    for (int i = 0; i < 12; ++i) osave[i] = o[i];
#pragma unroll
    for (int g = 0; g < 4; ++g) {
      f32x16 n0, n1;
      const char* kb;
      if (g == 3) {
        if (tile < 7) {
          wait_vm0();
          barrier_raw();
          if (tile < 6) stage((nxt == 2 * AT_STAGE) ? 0 : nxt + AT_STAGE, tile + 2);
        }
        kb = smem + nxt;
      } else {
        kb = smem + cur + (g + 1) * 3072;
      }
      n0 = MFMA32(*(const bf16x8*)(kb + k1_off), q1, zero);
      n0 = MFMA32(*(const bf16x8*)(kb + k2_off), __builtin_bit_cast(bf16x8, q2), n0);
      n1 = MFMA32(*(const bf16x8*)(kb + 1536 + k1_off), q1, zero);
      n1 = MFMA32(*(const bf16x8*)(kb + 1536 + k2_off), __builtin_bit_cast(bf16x8, q2), n1);
#pragma unroll
      for (int s = 0; s < 4; ++s) {
        const f32x16& sv = (s < 2) ? s0 : s1;
        u32x4 pv;
#pragma unroll
        for (int i = 0; i < 4; ++i)
          pv[i] = pack_bf2(__builtin_amdgcn_exp2f(sv[(s & 1) * 8 + 2 * i]), __builtin_amdgcn_exp2f(sv[(s & 1) * 8 + 2 * i + 1]));
        bf16x8 vf = *(const bf16x8*)(smem + cur + (g * 4 + s) * 768 + v_off);
        o = MFMA32(vf, __builtin_bit_cast(bf16x8, pv), o);
      }
      s0 = n0; s1 = n1;
      __builtin_amdgcn_sched_group_barrier(0x100, 6, 0);
#pragma unroll
      for (int k = 0; k < 4; ++k) {
        __builtin_amdgcn_sched_group_barrier(0x400, 4, 0);
        __builtin_amdgcn_sched_group_barrier(0x008, 1, 0);
      }
      __builtin_amdgcn_sched_barrier(0);
    }
    if (__builtin_amdgcn_ballot_w64(h == 1 && !(o[8] < AT_LIMIT)) != 0) {
      float mx = -3.0e38f;
#pragma unroll 1
      for (int hf = 0; hf < 8; ++hf) {
        const char* kc = smem + cur + hf * 1536;
        f32x16 t = MFMA32(*(const bf16x8*)(kc + k1_off), q1, zero);
        t = MFMA32(*(const bf16x8*)(kc + k2_off), __builtin_bit_cast(bf16x8, q2), t);
#pragma unroll
        for (int i = 0; i < 16; ++i) mx = fmaxf(mx, t[i]);
      }
      mx = fmaxf(mx, __shfl_xor(mx, 32));
      const float mnew = rbf(mref + fmaxf(mx + AT_BIAS, 0.f));
      const float delta = mnew - mref;
      const float alpha = __builtin_amdgcn_exp2f(-delta);
      mref = mnew;
      if (h == 0) q2[2] = pack_bf2(-mref, 0.f);
#pragma unroll
      for (int i = 0; i < 16; ++i) { s0[i] -= delta; s1[i] -= delta; o[i] = (i < 12) ? osave[i] * alpha : 0.f; }
#pragma unroll 1
      for (int hf = 0; hf < 8; ++hf) {
        const char* kc = smem + cur + hf * 1536;
        f32x16 t = MFMA32(*(const bf16x8*)(kc + k1_off), q1, zero);
        t = MFMA32(*(const bf16x8*)(kc + k2_off), __builtin_bit_cast(bf16x8, q2), t);
#pragma unroll
        for (int s = 0; s < 2; ++s) {
          u32x4 pv;
#pragma unroll
          for (int i = 0; i < 4; ++i)
            pv[i] = pack_bf2(__builtin_amdgcn_exp2f(t[s * 8 + 2 * i]), __builtin_amdgcn_exp2f(t[s * 8 + 2 * i + 1]));
          bf16x8 vf = *(const bf16x8*)(smem + cur + (hf * 2 + s) * 768 + v_off);
          o = MFMA32(vf, __builtin_bit_cast(bf16x8, pv), o);
        }
      }
    }
    cur = nxt;
  }
  const float l = __shfl(o[8], r + 32);
  const float inv = 1.f / l;
  auto ostore = [&](int d, u32x2 w) {
    const int col = head * DH + d;
    *(u32x2*)(obuf + ((size_t)((q >> 4) * 20 + (col >> 5)) * 64 + ((col >> 3) & 3) * 16 + (q & 15)) * 8 + (col & 7)) = w;
  };
  {
    u32x2 w0 = {pack_bf2(o[0] * inv, o[1] * inv), pack_bf2(o[2] * inv, o[3] * inv)};
    u32x2 w1 = {pack_bf2(o[4] * inv, o[5] * inv), pack_bf2(o[6] * inv, o[7] * inv)};
    ostore(4 * h, w0);
    ostore(4 * h + 8, w1);
    if (h == 0) {
      u32x2 w2 = {pack_bf2(o[8] * inv, o[9] * inv), pack_bf2(o[10] * inv, o[11] * inv)};
      ostore(16, w2);
    }
  }
}

DI void down_role(char* smem, int b, const u16* __restrict__ hbuf, const u16* __restrict__ Bf,
                  const u16* __restrict__ x, float* __restrict__ y) {
  const int t = (b & 7) * 32 + (b >> 3);
  const int nblk = t & 7, mblk = t >> 3;
  const int tid = threadIdx.x, wave = tid >> 6, lane = tid & 63;
  const int m0 = mblk * 64, n0 = nblk * 80;
  const unsigned wv = __builtin_amdgcn_readfirstlane(wave);
  const int mh = wave & 1, ks = wave >> 1;
  const u16* ap = hbuf + ((size_t)((mblk * 4 + mh * 2) * 54 + ks) * 64 + lane) * 8;
  const u16* bp = Bf + ((size_t)((nblk * 54 + ks) * 5) * 64 + lane) * 8;
  f32x4 acc[2][5];
#pragma unroll
  for (int i = 0; i < 2; ++i)
#pragma unroll
    for (int j = 0; j < 5; ++j) acc[i][j] = (f32x4){0.f, 0.f, 0.f, 0.f};
  bf16x8 a0[2], b0[5], a1[2], b1[5];
  auto load = [&](bf16x8 (&af)[2], bf16x8 (&bfr)[5], int it) {
    af[0] = *(const bf16x8*)(ap + it * 4 * 512);
    af[1] = *(const bf16x8*)(ap + 54 * 512 + it * 4 * 512);
#pragma unroll
    for (int j = 0; j < 5; ++j) bfr[j] = *(const bf16x8*)(bp + (it * 20 + j) * 512);
  };
  auto mma = [&](bf16x8 (&af)[2], bf16x8 (&bfr)[5]) {
#pragma unroll
    for (int j = 0; j < 5; ++j) {
      acc[0][j] = MFMA16(bfr[j], af[0], acc[0][j]);
      acc[1][j] = MFMA16(bfr[j], af[1], acc[1][j]);
    }
  };
  load(a0, b0, 0);
  for (int it = 0; it < 12; it += 2) {
    load(a1, b1, it + 1);
    mma(a0, b0);
    load(a0, b0, it + 2);
    mma(a1, b1);
  }
  if (wv < 4) load(a1, b1, 13);
  mma(a0, b0);
  if (wv < 4) mma(a1, b1);
  if (wv >= 2) {
    f32x4* red = (f32x4*)smem + (wave - 2) * 640 + lane;
#pragma unroll
    for (int i = 0; i < 2; ++i)
#pragma unroll
      for (int j = 0; j < 5; ++j) red[(i * 5 + j) * 64] = acc[i][j];
  }
  barrier_raw();
  if (wv < 2) {
#pragma unroll
    for (int i = 0; i < 2; ++i) {
      const size_t gi = (size_t)(m0 + mh * 32 + i * 16 + (lane & 15)) * DM + n0 + (lane >> 4) * 4;
#pragma unroll
      for (int j = 0; j < 5; ++j) {
        f32x4 s = acc[i][j];
#pragma unroll
        for (int w = 0; w < 3; ++w) s += ((const f32x4*)smem)[(w * 2 + mh) * 640 + (i * 5 + j) * 64 + lane];
        u32x2 xv = *(const u32x2*)(x + gi + j * 16);
        s[0] += bf_lo(xv[0]); s[1] += bf_hi(xv[0]); s[2] += bf_lo(xv[1]); s[3] += bf_hi(xv[1]);
        *(f32x4*)(y + ((size_t)(t * 20 + mh * 10 + i * 5 + j) * 64 + lane) * 4) = s;
      }
    }
  }
}

__global__ __launch_bounds__(512, 4) void k2_kernel(const u16* __restrict__ Qg, const u16* __restrict__ Kf,
                                                    const u16* __restrict__ Vf, u16* __restrict__ obuf,
                                                    const u16* __restrict__ hbuf, const u16* __restrict__ Bf,
                                                    const u16* __restrict__ x, float* __restrict__ y) {
  __shared__ __attribute__((aligned(16))) char smem[3 * AT_STAGE];
  const int b = blockIdx.x;
  if (b < 256) attn_role(smem, b, Qg, Kf, Vf, obuf);
  else down_role(smem, b - 256, hbuf, Bf, x, y);
}

__global__ __launch_bounds__(512, 2) void k3_kernel(const u16* __restrict__ obuf, const u16* __restrict__ Bt,
                                                    const float* __restrict__ y, u16* __restrict__ out) {
  __shared__ __attribute__((aligned(16))) char smem[80 * 1280];
  const int b = blockIdx.x;
  const int t = (b & 7) * 32 + (b >> 3);
  const int nblk = t & 7, mblk = t >> 3;
  const int tid = threadIdx.x, wave = tid >> 6, lane = tid & 63;
  const int m0 = mblk * 64, n0 = nblk * 80;
  const unsigned lds0 = __builtin_amdgcn_readfirstlane((unsigned)(size_t)smem);
  const unsigned wv = __builtin_amdgcn_readfirstlane(wave);
  {
    const unsigned b_vo = nblk * 80 * 1280 + wave * 1024 + lane * 16;
#pragma unroll
    for (int i = 0; i < 13; ++i)
      if (i < 12 || wv < 4) glds16s(Bt + i * 4096, b_vo, lds0 + (i * 8 + wv) * 1024);
  }
  const int mh = wave & 1, ks = wave >> 1;
  bf16x8 af[5][2];
  {
    const u16* ap = obuf + ((size_t)((mblk * 4 + mh * 2) * 20 + ks) * 64 + lane) * 8;
#pragma unroll
    for (int s = 0; s < 5; ++s) {
      af[s][0] = *(const bf16x8*)(ap + s * 4 * 512);
      af[s][1] = *(const bf16x8*)(ap + 20 * 512 + s * 4 * 512);
    }
  }
  f32x4 yv[3];
  {
    const float* yp = y + ((size_t)(t * 20 + wave) * 64 + lane) * 4;
    yv[0] = *(const f32x4*)(yp);
    yv[1] = *(const f32x4*)(yp + 8 * 256);
    yv[2] = *(const f32x4*)(yp + (wv < 4 ? 16 : 8) * 256);
  }
  f32x4 acc[2][5];
#pragma unroll
  for (int i = 0; i < 2; ++i)
#pragma unroll
    for (int j = 0; j < 5; ++j) acc[i][j] = (f32x4){0.f, 0.f, 0.f, 0.f};
  int b_off;
  {
    int g = lane >> 4, q = (lane & 15) >> 2, p = lane & 3;
    b_off = (ks * 4 + g) * 1280 + ((g & 1) << 7) + q * 32 + p * 8;
  }
  wait_vm0();
  barrier_raw();
#pragma unroll
  for (int s = 0; s < 5; ++s) {
    bf16x8 bfr[5];
#pragma unroll
    for (int j = 0; j < 5; ++j) {
      s16x4 lo = tr_read(smem + b_off + s * 20480 + j * 256);
      s16x4 hi = tr_read(smem + (b_off ^ 128) + s * 20480 + j * 256);
      bfr[j] = __builtin_shufflevector(lo, hi, 0, 1, 2, 3, 4, 5, 6, 7);
    }
#pragma unroll
    for (int j = 0; j < 5; ++j) {
      acc[0][j] = MFMA16(bfr[j], af[s][0], acc[0][j]);
      acc[1][j] = MFMA16(bfr[j], af[s][1], acc[1][j]);
    }
  }
  barrier_raw();
  {
    f32x4* red = (f32x4*)smem + (ks * 20 + mh * 10) * 64 + lane;
#pragma unroll
    for (int i = 0; i < 2; ++i)
#pragma unroll
      for (int j = 0; j < 5; ++j) red[(i * 5 + j) * 64] = acc[i][j];
  }
  barrier_raw();
#pragma unroll
  for (int u = 0; u < 3; ++u) {
    const int tile = wave + u * 8;
    if (u < 2 || wv < 4) {
      f32x4 s = yv[u];
#pragma unroll
      for (int w = 0; w < 4; ++w) s += ((const f32x4*)smem)[(w * 20 + tile) * 64 + lane];
      const int rt = tile / 5, j = tile - rt * 5;
      u32x2 o = {pack_bf2(s[0], s[1]), pack_bf2(s[2], s[3])};
      *(u32x2*)(out + (size_t)(m0 + rt * 16 + (lane & 15)) * DM + n0 + j * 16 + (lane >> 4) * 4) = o;
    }
  }
}

extern "C" void kernel_launch(void* const* d_in, const int* in_sizes, int n_in, void* d_out, int out_size,
                              void* d_ws, size_t ws_size, hipStream_t stream) {
  const u16* x = (const u16*)d_in[0];
  const u16* wqkv = (const u16*)d_in[1];
  const u16* wo = (const u16*)d_in[2];
  const u16* wg = (const u16*)d_in[4];
  const u16* wu = (const u16*)d_in[5];
  const u16* wd = (const u16*)d_in[6];
  char* ws = (char*)d_ws;
  u16* Qg = (u16*)(ws + (4u << 20));
  u16* Kf = (u16*)(ws + (8u << 20));
  u16* Vf = (u16*)(ws + (12u << 20));
  u16* hb = (u16*)(ws + (16u << 20));
  u16* ob = (u16*)(ws + (24u << 20));
  float* yb = (float*)(ws + (28u << 20));
  u16* Bt = (u16*)(ws + (34u << 20));
  u16* Bf = (u16*)(ws + (35u << 20));
  gemm1_kernel<<<256, 512, 0, stream>>>(x, wqkv, wg, wu, Qg, Kf, Vf, hb, wd, wo, Bt, Bf);
  k2_kernel<<<512, 512, 0, stream>>>(Qg, Kf, Vf, ob, hb, Bf, x, yb);
  k3_kernel<<<256, 512, 0, stream>>>(ob, Bt, yb, (u16*)d_out);
}
```

```cpp
#include <hip/hip_runtime.h>
#include <stdint.h>

typedef __attribute__((ext_vector_type(8))) short bf16x8;
typedef __attribute__((ext_vector_type(4))) short s16x4;
typedef __attribute__((ext_vector_type(4))) float f32x4;
typedef __attribute__((ext_vector_type(2))) float f32x2;
typedef __attribute__((ext_vector_type(16))) float f32x16;
typedef __attribute__((ext_vector_type(2))) __bf16 bf2_t;
typedef __attribute__((ext_vector_type(8))) __bf16 bf8_t;
typedef __attribute__((ext_vector_type(4))) unsigned u32x4;
typedef __attribute__((ext_vector_type(2))) unsigned u32x2;
typedef unsigned short u16;
typedef __attribute__((address_space(3))) s16x4 lds_s16x4;

#define DI __device__ __forceinline__

constexpr int SEQ = 2048, DM = 640, NH = 32, DH = 20, DF = 1728;

DI unsigned pack_bf2(float a, float b) {
  f32x2 v = {a, b};
  return __builtin_bit_cast(unsigned, __builtin_convertvector(v, bf2_t));
}
DI float bf_lo(unsigned u) { return __uint_as_float(u << 16); }
DI float bf_hi(unsigned u) { return __uint_as_float(u & 0xffff0000u); }
DI float rbf(float x) { return bf_lo(pack_bf2(x, 0.f)); }

DI void glds16(const void* g, unsigned lds_addr) {
  unsigned keep;
  asm volatile("s_mov_b32 %0, m0\n\ts_mov_b32 m0, %2\n\ts_nop 0\n\tglobal_load_lds_dwordx4 %1, off\n\ts_mov_b32 m0, %0"
               : "=&s"(keep) : "v"(g), "s"(lds_addr) : "memory");
}
DI void glds16s(const void* sbase, unsigned voff, unsigned lds_addr) {
  unsigned keep;
  asm volatile("s_mov_b32 %0, m0\n\ts_mov_b32 m0, %3\n\ts_nop 0\n\tglobal_load_lds_dwordx4 %1, %2\n\ts_mov_b32 m0, %0"
               : "=&s"(keep) : "v"(voff), "s"(sbase), "s"(lds_addr) : "memory");
}
DI void wait_vm0() { asm volatile("s_waitcnt vmcnt(0)" ::: "memory"); }
DI void barrier_raw() {
  asm volatile("s_waitcnt lgkmcnt(0)" ::: "memory");
  __builtin_amdgcn_s_barrier();
  asm volatile("" ::: "memory");
}
template <int V> struct IC { static constexpr int value = V; };
template <int N, int I = 0, class F> DI void static_for(F&& f) {
  if constexpr (I < N) { f(IC<I>{}); static_for<N, I + 1>(f); }
}
DI s16x4 tr_read(const char* lds) {
  return __builtin_amdgcn_ds_read_tr16_b64_v4i16((lds_s16x4*)lds);
}

constexpr int G1_BOFF = 3 * 32768;
__global__ __launch_bounds__(512, 2) void gemm1_kernel(const u16* __restrict__ x, const u16* __restrict__ wqkv,
                                                       const u16* __restrict__ wg, const u16* __restrict__ wu,
                                                       u16* __restrict__ Qg, u16* __restrict__ Kf,
                                                       u16* __restrict__ Vf, u16* __restrict__ hbuf,
                                                       const u16* __restrict__ wd, const u16* __restrict__ wo,
                                                       u16* __restrict__ Bt, u16* __restrict__ Bf) {
  __shared__ __attribute__((aligned(16))) char smem[G1_BOFF + 2 * 24576];
  const int b = blockIdx.x;
  if (b >= 224) {
    const int tsp = (b - 224) * 512 + threadIdx.x;
    {
      u32x4 v[4];
#pragma unroll
      for (int u = 0; u < 4; ++u) {
        int it = tsp + u * 16384;
        if (it >= 640 * 80) it = 0;
        const int k = it / 80, c8 = it - k * 80;
        v[u] = *(const u32x4*)(wo + (size_t)k * DM + c8 * 8);
      }
#pragma unroll
      for (int u = 0; u < 4; ++u) {
        const int it = tsp + u * 16384;
        const int k = it / 80, c8 = it - k * 80;
        const int nb = c8 / 10, r10 = c8 - nb * 10, kb = k >> 3;
        u16* dst = Bt + ((size_t)(nb * 80 + kb) * 640 + (r10 >> 1) * 128 + ((k & 7) ^ ((kb & 1) << 2)) * 16 + (r10 & 1) * 8);
        if (it < 640 * 80) *(u32x4*)dst = v[u];
      }
    }
    for (int it = tsp; it < 216 * 640; it += 16384) {
      const int kb = it / 640, n = it - kb * 640;
      const u16* src = wd + (size_t)kb * 8 * DM + n;
      unsigned e[8];
#pragma unroll
      for (int j = 0; j < 8; ++j) e[j] = src[j * DM];
      const int nb = n / 80, r = n - nb * 80;
      u32x4 w = {e[0] | (e[1] << 16), e[2] | (e[3] << 16), e[4] | (e[5] << 16), e[6] | (e[7] << 16)};
      *(u32x4*)(Bf + ((size_t)((nb * 54 + (kb >> 2)) * 5 + (r >> 4)) * 64 + (kb & 3) * 16 + (r & 15)) * 8) = w;
    }
    return;
  }
  const int t = (b & 7) * 28 + (b >> 3);
  const int nblk = t >> 3, mblk = t & 7;
  const int tid = threadIdx.x, wave = tid >> 6, lane = tid & 63;
  const int wm = wave >> 1, wn = wave & 1;
  const int m0 = mblk * 256;
  const bool isqkv = nblk < 10;

  const unsigned a_vo = ((m0 + wave * 32 + (lane >> 3)) * DM + ((lane & 7) ^ (lane >> 4)) * 8) * 2;
  const int ldb = isqkv ? 3 * DM : DF;
  const u16* b_src[3];
#pragma unroll
  for (int i = 0; i < 3; ++i) {
    int kb = wave;
    int nt = i * 4 + (lane >> 4);
    int rr = ((lane >> 1) & 7) ^ ((kb & 1) << 2);
    int k = kb * 8 + rr;
    const u16* base;
    if (isqkv) base = wqkv + nblk * 192 + nt * 16;
    else base = (nt < 6) ? (wg + (nblk - 10) * 96 + nt * 16) : (wu + (nblk - 10) * 96 + (nt - 6) * 16);
    b_src[i] = base + (size_t)k * ldb + (lane & 1) * 8;
  }
  const unsigned lds0 = __builtin_amdgcn_readfirstlane((unsigned)(size_t)smem);
  const unsigned wv = __builtin_amdgcn_readfirstlane(wave);
  auto stage_a = [&](int kt, int g) {
    glds16s(x + g * 8 * DM + kt * 64, a_vo ^ ((g & 1) << 6), lds0 + (kt % 3) * 32768 + (wv * 4 + g) * 1024);
  };
  auto stage_b = [&](int kt, int g) {
    glds16(b_src[g] + (size_t)kt * 64 * ldb, lds0 + G1_BOFF + (kt & 1) * 24576 + (wv * 3 + g) * 1024);
  };
#pragma unroll
  for (int kt = 0; kt < 2; ++kt) {
#pragma unroll
    for (int g = 0; g < 4; ++g) stage_a(kt, g);
#pragma unroll
    for (int g = 0; g < 3; ++g) stage_b(kt, g);
  }

  const int a_off = ((wm * 64 + wn * 32 + (lane & 15)) * 128 + (((lane >> 4) ^ ((lane >> 1) & 7)) << 4)) ^ ((wm & 1) << 6);
  int b_off[2][2];
  {
    int g = lane >> 4, q = (lane & 15) >> 2, p = lane & 3;
    int base = G1_BOFF + g * 3072 + ((g & 1) << 7) + q * 32 + p * 8;
    int t0 = (isqkv ? wn * 6 : wn * 3) * 256, t1 = (isqkv ? wn * 6 + 3 : 6 + wn * 3) * 256;
    if (wm >= 2) { int tt = t0; t0 = t1; t1 = tt; }
#pragma unroll
    for (int kk = 0; kk < 2; ++kk) {
      b_off[kk][0] = base + t0 + (kk ^ (wm & 1)) * 12288;
      b_off[kk][1] = base + t1 + (kk ^ (wm & 1)) * 12288;
    }
  }

  f32x4 acc[4][6];
#pragma unroll
  for (int i = 0; i < 4; ++i)
#pragma unroll
    for (int j = 0; j < 6; ++j) acc[i][j] = (f32x4){0.f, 0.f, 0.f, 0.f};

  bf16x8 af0[4], bf0[6], af1[4], bf1[6];
  auto read_q = [&](bf16x8 (&af)[4], bf16x8 (&bfr)[6], int kt, int kk, int g) {
    const char* sa = smem + (kt % 3) * 32768;
    const char* sb = smem + (kt & 1) * 24576;
    auto rb = [&](int j) {
      s16x4 lo = tr_read(sb + b_off[kk][j / 3] + (j % 3) * 256);
      s16x4 hi = tr_read(sb + (b_off[kk][j / 3] ^ 128) + (j % 3) * 256);
      bfr[j] = __builtin_shufflevector(lo, hi, 0, 1, 2, 3, 4, 5, 6, 7);
    };
    auto ra = [&](int i) { af[i] = *(const bf16x8*)(sa + ((a_off ^ (kk << 6)) ^ ((i >> 1) << 12)) + (i & 1) * 2048); };
    if (g == 0) { ra(0); rb(0); rb(1); }
    if (g == 1) { rb(2); rb(3); }
    if (g == 2) { rb(4); rb(5); }
    if (g == 3) { ra(1); ra(2); ra(3); }
  };
  auto mma_q = [&](bf16x8 (&af)[4], bf16x8 (&bfr)[6], int i) {
#pragma unroll
    for (int j = 0; j < 6; ++j)
      acc[i][j] = __builtin_amdgcn_mfma_f32_16x16x32_bf16(bfr[j], af[i], acc[i][j], 0, 0, 0);
  };

  f32x4 rs[2], rq[2], cs[3];
#pragma unroll
  for (int j = 0; j < 2; ++j) { rs[j] = (f32x4){0.f, 0.f, 0.f, 0.f}; rq[j] = (f32x4){0.f, 0.f, 0.f, 0.f}; }
#pragma unroll
  for (int j = 0; j < 3; ++j) cs[j] = (f32x4){0.f, 0.f, 0.f, 0.f};
  const bf16x8 ones = {0x3F80, 0x3F80, 0x3F80, 0x3F80, 0x3F80, 0x3F80, 0x3F80, 0x3F80};
  auto stats_q = [&](bf16x8 (&af)[4], bf16x8 (&bfr)[6], int kk, int g) {
    if (g < 2) {
      rs[g] = __builtin_amdgcn_mfma_f32_16x16x32_bf16(ones, af[g], rs[g], 0, 0, 0);
      rq[g] = __builtin_amdgcn_mfma_f32_16x16x32_bf16(af[g], af[g], rq[g], 0, 0, 0);
    }
    if (g < 3 && kk == 0) cs[g] = __builtin_amdgcn_mfma_f32_16x16x32_bf16(bfr[g], ones, cs[g], 0, 0, 0);
  };

  asm volatile("s_waitcnt vmcnt(7)" ::: "memory");
  barrier_raw();
#pragma unroll
  for (int g = 0; g < 4; ++g) read_q(af0, bf0, 0, 0, g);
#pragma unroll
  for (int kt = 0; kt < 10; ++kt) {
#pragma unroll
    for (int g = 0; g < 4; ++g) {
      __builtin_amdgcn_sched_barrier(0);
      read_q(af1, bf1, kt, 1, g);
      mma_q(af0, bf0, g);
      stats_q(af0, bf0, 0, g);
      __builtin_amdgcn_sched_barrier(0);
      if (kt + 2 < 10) stage_a(kt + 2, g);
    }
    __builtin_amdgcn_sched_barrier(0);
    if (kt + 1 < 10) {
      if (kt + 2 < 10) asm volatile("s_waitcnt vmcnt(4)" ::: "memory");
      else wait_vm0();
      barrier_raw();
    }
#pragma unroll
    for (int g = 0; g < 4; ++g) {
      __builtin_amdgcn_sched_barrier(0);
      if (kt + 1 < 10) read_q(af0, bf0, kt + 1, 0, g);
      mma_q(af1, bf1, g);
      stats_q(af1, bf1, 1, g);
      __builtin_amdgcn_sched_barrier(0);
      if (kt + 2 < 10 && g < 3) stage_b(kt + 2, g);
    }
  }
  __builtin_amdgcn_sched_barrier(0);
  barrier_raw();

  int tz = threadIdx.x;
  asm volatile("" : "+v"(tz));
  const int e_wave = __builtin_amdgcn_readfirstlane(tz >> 6), e_lane = tz & 63, e_wm = e_wave >> 1, e_wn = e_wave & 1;
  const int ln4 = (e_lane >> 4) * 4;
  {
    f32x2* st_row = (f32x2*)(smem + 65536);
    float* st_col = (float*)(smem + 65536 + 4096);
#pragma unroll
    for (int i = 0; i < 2; ++i) {
      const int m = e_lane & 15;
      const float q = (m & 2) ? ((m & 1) ? rq[i][3] : rq[i][2]) : ((m & 1) ? rq[i][1] : rq[i][0]);
      if ((e_lane >> 4) == (m >> 2)) st_row[e_wm * 64 + e_wn * 32 + i * 16 + m] = (f32x2){rs[i][0], q};
    }
    if ((e_lane & 15) == 0) {
#pragma unroll
      for (int j = 0; j < 3; ++j)
        *(f32x4*)(st_col + ((e_wm & 1) * 2 + e_wn) * 96 + ((e_wm >> 1) * 3 + j) * 16 + ln4) = cs[j];
    }
    barrier_raw();
    float mb[4], inv[4];
#pragma unroll
    for (int i = 0; i < 4; ++i) {
      const f32x2 p0 = st_row[e_wm * 64 + ((i + 2 * e_wn) & 3) * 16 + (e_lane & 15)];
      const float mean = p0[0] * (1.f / DM);
      const float var = fmaxf(p0[1] * (1.f / DM) - mean * mean, 0.f);
      mb[i] = rbf(mean);
      inv[i] = __builtin_amdgcn_rcpf(rbf(__builtin_amdgcn_sqrtf(rbf(rbf(var) + 1e-5f))));
    }
#pragma unroll
    for (int j = 0; j < 6; ++j) {
      const int jl = (e_wm >= 2) ? (j + 3) % 6 : j;
      const f32x4 c = *(const f32x4*)(st_col + e_wn * 96 + jl * 16 + ln4) + *(const f32x4*)(st_col + (2 + e_wn) * 96 + jl * 16 + ln4);
#pragma unroll
      for (int i = 0; i < 4; ++i)
#pragma unroll
        for (int r = 0; r < 4; ++r) acc[i][j][r] = __builtin_fmaf(-mb[i], c[r], acc[i][j][r]) * inv[i];
    }
  }
  if (isqkv) {
    const float qscale = 0.22360679774997896f * 1.4426950408889634f;
    char* wtr = smem + e_wave * 512;
#pragma unroll
    for (int j = 0; j < 6; ++j) {
      const int nb = nblk * 192 + (e_wn * 6 + ((e_wm >= 2) ? (j + 3) % 6 : j)) * 16;
      const int sec = nb / DM;
      if (sec < 2) {
        const int rr = nb + ln4 - sec * DM, head = rr / DH, d = rr - head * DH, d4 = d >> 2;
#pragma unroll
        for (int i = 0; i < 4; ++i) {
          const int tok = m0 + e_wm * 64 + ((i + 2 * e_wn) & 3) * 16 + (e_lane & 15);
          f32x4 v = acc[i][j];
          if (sec == 0) {
            u32x2 o = {pack_bf2(v[0] * qscale, v[1] * qscale), pack_bf2(v[2] * qscale, v[3] * qscale)};
            *(u32x2*)(Qg + ((size_t)((tok >> 4) * 40 + (nb >> 4)) * 64 + e_lane) * 4) = o;
          } else {
            char* kb = (char*)Kf + (size_t)(head * 64 + (tok >> 5)) * 1536;
            const int r = tok & 31;
            unsigned p0 = pack_bf2(v[0], v[1]), p1 = pack_bf2(v[2], v[3]);
            if (d4 < 4) {
              u32x2 o = {p0, p1};
              *(u32x2*)(kb + ((d4 >> 1) * 32 + r) * 16 + (d4 & 1) * 8) = o;
            } else {
              u32x4 o = {p0, p1, 0x00003F80u, 0u};
              *(u32x4*)(kb + 1024 + r * 16) = o;
            }
          }
        }
      } else {
        const int rr = nb + (e_lane & 15) - 2 * DM, head = rr / DH, d = rr - head * DH;
        const int g = e_lane >> 4;
#pragma unroll
        for (int i = 0; i < 4; ++i) {
          f32x4 v = acc[i][j];
          u32x2 w = {pack_bf2(v[0], v[1]), pack_bf2(v[2], v[3])};
          *(u32x2*)(wtr + (e_lane & 15) * 32 + g * 8) = w;
          s16x4 tv = tr_read(wtr + g * 128 + ((e_lane & 15) >> 2) * 32 + (e_lane & 3) * 8);
          const int s = (m0 + e_wm * 64 + ((i + 2 * e_wn) & 3) * 16) >> 4;
          u16* vb = Vf + ((size_t)(head * 128 + s) * 48 + (g & 1) * 24 + d) * 8 + (g >> 1) * 4;
          *(s16x4*)vb = tv;
          if (d == DH - 1) { s16x4 one = {0x3F80, 0x3F80, 0x3F80, 0x3F80}; *(s16x4*)(vb + 8) = one; }
        }
      }
    }
  } else {
#pragma unroll
    for (int j = 0; j < 3; ++j) {
      const int f = (nblk - 10) * 96 + (e_wn * 3 + j) * 16 + ln4;
#pragma unroll
      for (int i = 0; i < 4; ++i) {
        const int tok = m0 + e_wm * 64 + ((i + 2 * e_wn) & 3) * 16 + (e_lane & 15);
        float hv[4];
#pragma unroll
        for (int r = 0; r < 4; ++r) {
          const float g = (e_wm >= 2) ? acc[i][j + 3][r] : acc[i][j][r], u = (e_wm >= 2) ? acc[i][j][r] : acc[i][j + 3][r];
          const float e = __builtin_amdgcn_exp2f(g * __builtin_fmaf(g * g, -0.10294324f, -2.30220819f));
          hv[r] = (g * u) * __builtin_amdgcn_rcpf(1.f + e);
        }
        u32x2 o = {pack_bf2(hv[0], hv[1]), pack_bf2(hv[2], hv[3])};
        *(u32x2*)(hbuf + ((size_t)((tok >> 4) * 54 + (f >> 5)) * 64 + ((f >> 3) & 3) * 16 + (tok & 15)) * 8 + (f & 7)) = o;
      }
    }
  }
}

constexpr int AT_STAGE = 24576;
#define AT_BIAS_V 8.f
constexpr float AT_BIAS = AT_BIAS_V;
#define AT_LIMIT_V 1073741824.f
constexpr float AT_LIMIT = AT_LIMIT_V;
#define MFMA32(a, b, c) __builtin_amdgcn_mfma_f32_32x32x16_bf16((a), (b), (c), 0, 0, 0)
#define MFMA16(a, b, c) __builtin_amdgcn_mfma_f32_16x16x32_bf16((a), (b), (c), 0, 0, 0)

DI void attn_role(char* smem, int b, const u16* __restrict__ Qg, const u16* __restrict__ Kf,
                  const u16* __restrict__ Vf, u16* __restrict__ obuf) {
  const int head = (b & 7) * 4 + ((b >> 3) & 3), qb = b >> 5;
  const int tid = threadIdx.x, wave = tid >> 6, lane = tid & 63;
  const int r = lane & 31, h = lane >> 5;
  const int q = qb * 256 + wave * 32 + r;

  const unsigned lds0 = __builtin_amdgcn_readfirstlane((unsigned)(size_t)smem);
  const unsigned wv = __builtin_amdgcn_readfirstlane(wave);
  const char* gsrc = ((wv < 4) ? (const char*)Kf + wv * 3072 : (const char*)Vf + (wv - 4) * 3072) +
                     (size_t)head * 98304 + lane * 16;
  auto stage = [&](unsigned boff, int tile) {
#pragma unroll
    for (int i = 0; i < 3; ++i) glds16(gsrc + tile * 12288 + i * 1024, lds0 + boff + (wv * 3 + i) * 1024);
  };
  auto qload = [&](int d) {
    const int col = head * DH + d;
    return *(const u32x2*)(Qg + ((size_t)((q >> 4) * 40 + (col >> 4)) * 64 + ((col >> 2) & 3) * 16 + (q & 15)) * 4);
  };
  bf16x8 q1;
  u32x4 q2 = {0u, 0u, 0u, 0u};
  {
    u32x2 ta = qload(8 * h), tb = qload(8 * h + 4), t2 = qload(16);
    u32x4 t = {ta[0], ta[1], tb[0], tb[1]};
    q1 = __builtin_bit_cast(bf16x8, t);
    if (h == 0) { q2[0] = t2[0]; q2[1] = t2[1]; }
  }
  stage(0, 0);
  stage(AT_STAGE, 1);

  const int k1_off = lane * 16, k2_off = 1024 + r * 16;
  const int v_off = 12288 + (h * 24 + (r < 23 ? r : 23)) * 16;

  f32x16 o, s0, s1;
  const f32x16 zero = {0.f, 0.f, 0.f, 0.f, 0.f, 0.f, 0.f, 0.f, 0.f, 0.f, 0.f, 0.f, 0.f, 0.f, 0.f, 0.f};
  o = zero;
  float mref;

  asm volatile("s_waitcnt vmcnt(3)" ::: "memory");
  barrier_raw();
  {
    s0 = MFMA32(*(const bf16x8*)(smem + k1_off), q1, zero);
    s0 = MFMA32(*(const bf16x8*)(smem + k2_off), __builtin_bit_cast(bf16x8, q2), s0);
    s1 = MFMA32(*(const bf16x8*)(smem + 1536 + k1_off), q1, zero);
    s1 = MFMA32(*(const bf16x8*)(smem + 1536 + k2_off), __builtin_bit_cast(bf16x8, q2), s1);
    float mx = fmaxf(s0[0], s1[0]);
#pragma unroll
    for (int i = 1; i < 16; ++i) mx = fmaxf(mx, fmaxf(s0[i], s1[i]));
    mref = rbf(fmaxf(mx, __shfl_xor(mx, 32)) + AT_BIAS);
    if (h == 0) q2[2] = pack_bf2(-mref, 0.f);
#pragma unroll
    for (int i = 0; i < 16; ++i) { s0[i] -= mref; s1[i] -= mref; }
  }
  unsigned cur = 0;
  for (int tile = 0; tile < 8; ++tile) {
    const unsigned nxt = (cur == 2 * AT_STAGE) ? 0 : cur + AT_STAGE;
    float osave[12];
#pragma unroll
    for (int i = 0; i < 12; ++i) osave[i] = o[i];
#pragma unroll
    for (int g = 0; g < 4; ++g) {
      f32x16 n0, n1;
      const char* kb;
      if (g == 3) {
        if (tile < 7) {
          wait_vm0();
          barrier_raw();
          if (tile < 6) stage((nxt == 2 * AT_STAGE) ? 0 : nxt + AT_STAGE, tile + 2);
        }
        kb = smem + nxt;
      } else {
        kb = smem + cur + (g + 1) * 3072;
      }
      n0 = MFMA32(*(const bf16x8*)(kb + k1_off), q1, zero);
      n0 = MFMA32(*(const bf16x8*)(kb + k2_off), __builtin_bit_cast(bf16x8, q2), n0);
      n1 = MFMA32(*(const bf16x8*)(kb + 1536 + k1_off), q1, zero);
      n1 = MFMA32(*(const bf16x8*)(kb + 1536 + k2_off), __builtin_bit_cast(bf16x8, q2), n1);
#pragma unroll
      for (int s = 0; s < 4; ++s) {
        const f32x16& sv = (s < 2) ? s0 : s1;
        u32x4 pv;
#pragma unroll
        for (int i = 0; i < 4; ++i)
          pv[i] = pack_bf2(__builtin_amdgcn_exp2f(sv[(s & 1) * 8 + 2 * i]), __builtin_amdgcn_exp2f(sv[(s & 1) * 8 + 2 * i + 1]));
        bf16x8 vf = *(const bf16x8*)(smem + cur + (g * 4 + s) * 768 + v_off);
        o = MFMA32(vf, __builtin_bit_cast(bf16x8, pv), o);
      }
      s0 = n0; s1 = n1;
      __builtin_amdgcn_sched_group_barrier(0x100, 6, 0);
#pragma unroll
      for (int k = 0; k < 4; ++k) {
        __builtin_amdgcn_sched_group_barrier(0x400, 4, 0);
        __builtin_amdgcn_sched_group_barrier(0x008, 1, 0);
      }
      __builtin_amdgcn_sched_barrier(0);
    }
    if (__builtin_amdgcn_ballot_w64(h == 1 && !(o[8] < AT_LIMIT)) != 0) {
      float mx = -3.0e38f;
#pragma unroll 1
      for (int hf = 0; hf < 8; ++hf) {
        const char* kc = smem + cur + hf * 1536;
        f32x16 t = MFMA32(*(const bf16x8*)(kc + k1_off), q1, zero);
        t = MFMA32(*(const bf16x8*)(kc + k2_off), __builtin_bit_cast(bf16x8, q2), t);
#pragma unroll
        for (int i = 0; i < 16; ++i) mx = fmaxf(mx, t[i]);
      }
      mx = fmaxf(mx, __shfl_xor(mx, 32));
      const float mnew = rbf(mref + fmaxf(mx + AT_BIAS, 0.f));
      const float delta = mnew - mref;
      const float alpha = __builtin_amdgcn_exp2f(-delta);
      mref = mnew;
      if (h == 0) q2[2] = pack_bf2(-mref, 0.f);
#pragma unroll
      for (int i = 0; i < 16; ++i) { s0[i] -= delta; s1[i] -= delta; o[i] = (i < 12) ? osave[i] * alpha : 0.f; }
#pragma unroll 1
      for (int hf = 0; hf < 8; ++hf) {
        const char* kc = smem + cur + hf * 1536;
        f32x16 t = MFMA32(*(const bf16x8*)(kc + k1_off), q1, zero);
        t = MFMA32(*(const bf16x8*)(kc + k2_off), __builtin_bit_cast(bf16x8, q2), t);
#pragma unroll
        for (int s = 0; s < 2; ++s) {
          u32x4 pv;
#pragma unroll
          for (int i = 0; i < 4; ++i)
            pv[i] = pack_bf2(__builtin_amdgcn_exp2f(t[s * 8 + 2 * i]), __builtin_amdgcn_exp2f(t[s * 8 + 2 * i + 1]));
          bf16x8 vf = *(const bf16x8*)(smem + cur + (hf * 2 + s) * 768 + v_off);
          o = MFMA32(vf, __builtin_bit_cast(bf16x8, pv), o);
        }
      }
    }
    cur = nxt;
  }
  const float l = __shfl(o[8], r + 32);
  const float inv = 1.f / l;
  auto ostore = [&](int d, u32x2 w) {
    const int col = head * DH + d;
    *(u32x2*)(obuf + ((size_t)((q >> 4) * 20 + (col >> 5)) * 64 + ((col >> 3) & 3) * 16 + (q & 15)) * 8 + (col & 7)) = w;
  };
  {
    u32x2 w0 = {pack_bf2(o[0] * inv, o[1] * inv), pack_bf2(o[2] * inv, o[3] * inv)};
    u32x2 w1 = {pack_bf2(o[4] * inv, o[5] * inv), pack_bf2(o[6] * inv, o[7] * inv)};
    ostore(4 * h, w0);
    ostore(4 * h + 8, w1);
    if (h == 0) {
      u32x2 w2 = {pack_bf2(o[8] * inv, o[9] * inv), pack_bf2(o[10] * inv, o[11] * inv)};
      ostore(16, w2);
    }
  }
}

DI void down_role(char* smem, int b, const u16* __restrict__ hbuf, const u16* __restrict__ Bf,
                  const u16* __restrict__ x, float* __restrict__ y) {
  const int t = (b & 7) * 32 + (b >> 3);
  const int nblk = t & 7, mblk = t >> 3;
  const int tid = threadIdx.x, wave = tid >> 6, lane = tid & 63;
  const int m0 = mblk * 64, n0 = nblk * 80;
  const unsigned wv = __builtin_amdgcn_readfirstlane(wave);
  const int mh = wave & 1, ks = wave >> 1;
  const u16* ap = hbuf + ((size_t)((mblk * 4 + mh * 2) * 54 + ks) * 64 + lane) * 8;
  const u16* bp = Bf + ((size_t)((nblk * 54 + ks) * 5) * 64 + lane) * 8;
  f32x4 acc[2][5];
#pragma unroll
  for (int i = 0; i < 2; ++i)
#pragma unroll
    for (int j = 0; j < 5; ++j) acc[i][j] = (f32x4){0.f, 0.f, 0.f, 0.f};
  bf16x8 a0[2], b0[5], a1[2], b1[5];
  auto load = [&](bf16x8 (&af)[2], bf16x8 (&bfr)[5], int it) {
    af[0] = *(const bf16x8*)(ap + it * 4 * 512);
    af[1] = *(const bf16x8*)(ap + 54 * 512 + it * 4 * 512);
#pragma unroll
    for (int j = 0; j < 5; ++j) bfr[j] = *(const bf16x8*)(bp + (it * 20 + j) * 512);
  };
  auto mma = [&](bf16x8 (&af)[2], bf16x8 (&bfr)[5]) {
#pragma unroll
    for (int j = 0; j < 5; ++j) {
      acc[0][j] = MFMA16(bfr[j], af[0], acc[0][j]);
      acc[1][j] = MFMA16(bfr[j], af[1], acc[1][j]);
    }
  };
  load(a0, b0, 0);
  for (int it = 0; it < 12; it += 2) {
    load(a1, b1, it + 1);
    mma(a0, b0);
    load(a0, b0, it + 2);
    mma(a1, b1);
  }
  if (wv < 4) load(a1, b1, 13);
  mma(a0, b0);
  if (wv < 4) mma(a1, b1);
  if (wv >= 2) {
    f32x4* red = (f32x4*)smem + (wave - 2) * 640 + lane;
#pragma unroll
    for (int i = 0; i < 2; ++i)
#pragma unroll
      for (int j = 0; j < 5; ++j) red[(i * 5 + j) * 64] = acc[i][j];
  }
  barrier_raw();
  if (wv < 2) {
#pragma unroll
    for (int i = 0; i < 2; ++i) {
      const size_t gi = (size_t)(m0 + mh * 32 + i * 16 + (lane & 15)) * DM + n0 + (lane >> 4) * 4;
#pragma unroll
      for (int j = 0; j < 5; ++j) {
        f32x4 s = acc[i][j];
#pragma unroll
        for (int w = 0; w < 3; ++w) s += ((const f32x4*)smem)[(w * 2 + mh) * 640 + (i * 5 + j) * 64 + lane];
        u32x2 xv = *(const u32x2*)(x + gi + j * 16);
        s[0] += bf_lo(xv[0]); s[1] += bf_hi(xv[0]); s[2] += bf_lo(xv[1]); s[3] += bf_hi(xv[1]);
        *(f32x4*)(y + ((size_t)(t * 20 + mh * 10 + i * 5 + j) * 64 + lane) * 4) = s;
      }
    }
  }
}

__global__ __launch_bounds__(512, 4) void k2_kernel(const u16* __restrict__ Qg, const u16* __restrict__ Kf,
                                                    const u16* __restrict__ Vf, u16* __restrict__ obuf,
                                                    const u16* __restrict__ hbuf, const u16* __restrict__ Bf,
                                                    const u16* __restrict__ x, float* __restrict__ y) {
  __shared__ __attribute__((aligned(16))) char smem[3 * AT_STAGE];
  const int b = blockIdx.x;
  if (b < 256) attn_role(smem, b, Qg, Kf, Vf, obuf);
  else down_role(smem, b - 256, hbuf, Bf, x, y);
}

__global__ __launch_bounds__(512, 2) void k3_kernel(const u16* __restrict__ obuf, const u16* __restrict__ Bt,
                                                    const float* __restrict__ y, u16* __restrict__ out) {
  __shared__ __attribute__((aligned(16))) char smem[80 * 1280];
  const int b = blockIdx.x;
  const int t = (b & 7) * 32 + (b >> 3);
  const int nblk = t & 7, mblk = t >> 3;
  const int tid = threadIdx.x, wave = tid >> 6, lane = tid & 63;
  const int m0 = mblk * 64, n0 = nblk * 80;
  const unsigned lds0 = __builtin_amdgcn_readfirstlane((unsigned)(size_t)smem);
  const unsigned wv = __builtin_amdgcn_readfirstlane(wave);
  {
    const unsigned b_vo = nblk * 80 * 1280 + wave * 1024 + lane * 16;
#pragma unroll
    for (int i = 0; i < 13; ++i)
      if (i < 12 || wv < 4) glds16s(Bt + i * 4096, b_vo, lds0 + (i * 8 + wv) * 1024);
  }
  const int mh = wave & 1, ks = wave >> 1;
  bf16x8 af[5][2];
  {
    const u16* ap = obuf + ((size_t)((mblk * 4 + mh * 2) * 20 + ks) * 64 + lane) * 8;
#pragma unroll
    for (int s = 0; s < 5; ++s) {
      af[s][0] = *(const bf16x8*)(ap + s * 4 * 512);
      af[s][1] = *(const bf16x8*)(ap + 20 * 512 + s * 4 * 512);
    }
  }
  f32x4 yv[3];
  {
    const float* yp = y + ((size_t)(t * 20 + wave) * 64 + lane) * 4;
    yv[0] = *(const f32x4*)(yp);
    yv[1] = *(const f32x4*)(yp + 8 * 256);
    yv[2] = *(const f32x4*)(yp + (wv < 4 ? 16 : 8) * 256);
  }
  f32x4 acc[2][5];
#pragma unroll
  for (int i = 0; i < 2; ++i)
#pragma unroll
    for (int j = 0; j < 5; ++j) acc[i][j] = (f32x4){0.f, 0.f, 0.f, 0.f};
  int b_off;
  {
    int g = lane >> 4, q = (lane & 15) >> 2, p = lane & 3;
    b_off = (ks * 4 + g) * 1280 + ((g & 1) << 7) + q * 32 + p * 8;
  }
  wait_vm0();
  barrier_raw();
#pragma unroll
  for (int s = 0; s < 5; ++s) {
    bf16x8 bfr[5];
#pragma unroll
    for (int j = 0; j < 5; ++j) {
      s16x4 lo = tr_read(smem + b_off + s * 20480 + j * 256);
      s16x4 hi = tr_read(smem + (b_off ^ 128) + s * 20480 + j * 256);
      bfr[j] = __builtin_shufflevector(lo, hi, 0, 1, 2, 3, 4, 5, 6, 7);
    }
#pragma unroll
    for (int j = 0; j < 5; ++j) {
      acc[0][j] = MFMA16(bfr[j], af[s][0], acc[0][j]);
      acc[1][j] = MFMA16(bfr[j], af[s][1], acc[1][j]);
    }
  }
  barrier_raw();
  {
    f32x4* red = (f32x4*)smem + (ks * 20 + mh * 10) * 64 + lane;
#pragma unroll
    for (int i = 0; i < 2; ++i)
#pragma unroll
      for (int j = 0; j < 5; ++j) red[(i * 5 + j) * 64] = acc[i][j];
  }
  barrier_raw();
#pragma unroll
  for (int u = 0; u < 3; ++u) {
    const int tile = wave + u * 8;
    if (u < 2 || wv < 4) {
      f32x4 s = yv[u];
#pragma unroll
      for (int w = 0; w < 4; ++w) s += ((const f32x4*)smem)[(w * 20 + tile) * 64 + lane];
      const int rt = tile / 5, j = tile - rt * 5;
      u32x2 o = {pack_bf2(s[0], s[1]), pack_bf2(s[2], s[3])};
      *(u32x2*)(out + (size_t)(m0 + rt * 16 + (lane & 15)) * DM + n0 + j * 16 + (lane >> 4) * 4) = o;
    }
  }
}

extern "C" void kernel_launch(void* const* d_in, const int* in_sizes, int n_in, void* d_out, int out_size,
                              void* d_ws, size_t ws_size, hipStream_t stream) {
  const u16* x = (const u16*)d_in[0];
  const u16* wqkv = (const u16*)d_in[1];
  const u16* wo = (const u16*)d_in[2];
  const u16* wg = (const u16*)d_in[4];
  const u16* wu = (const u16*)d_in[5];
  const u16* wd = (const u16*)d_in[6];
  char* ws = (char*)d_ws;
  u16* Qg = (u16*)(ws + (4u << 20));
  u16* Kf = (u16*)(ws + (8u << 20));
  u16* Vf = (u16*)(ws + (12u << 20));
  u16* hb = (u16*)(ws + (16u << 20));
  u16* ob = (u16*)(ws + (24u << 20));
  float* yb = (float*)(ws + (28u << 20));
  u16* Bt = (u16*)(ws + (34u << 20));
  u16* Bf = (u16*)(ws + (35u << 20));
  gemm1_kernel<<<256, 512, 0, stream>>>(x, wqkv, wg, wu, Qg, Kf, Vf, hb, wd, wo, Bt, Bf);
  k2_kernel<<<512, 512, 0, stream>>>(Qg, Kf, Vf, ob, hb, Bf, x, yb);
  k3_kernel<<<256, 512, 0, stream>>>(ob, Bt, yb, (u16*)d_out);
}
```

```cpp
#include <hip/hip_runtime.h>
#include <stdint.h>

typedef __attribute__((ext_vector_type(8))) short bf16x8;
typedef __attribute__((ext_vector_type(4))) short s16x4;
typedef __attribute__((ext_vector_type(4))) float f32x4;
typedef __attribute__((ext_vector_type(2))) float f32x2;
typedef __attribute__((ext_vector_type(16))) float f32x16;
typedef __attribute__((ext_vector_type(2))) __bf16 bf2_t;
typedef __attribute__((ext_vector_type(8))) __bf16 bf8_t;
typedef __attribute__((ext_vector_type(4))) unsigned u32x4;
typedef __attribute__((ext_vector_type(2))) unsigned u32x2;
typedef unsigned short u16;
typedef __attribute__((address_space(3))) s16x4 lds_s16x4;

#define DI __device__ __forceinline__

constexpr int SEQ = 2048, DM = 640, NH = 32, DH = 20, DF = 1728;

DI unsigned pack_bf2(float a, float b) {
  f32x2 v = {a, b};
  return __builtin_bit_cast(unsigned, __builtin_convertvector(v, bf2_t));
}
DI float bf_lo(unsigned u) { return __uint_as_float(u << 16); }
DI float bf_hi(unsigned u) { return __uint_as_float(u & 0xffff0000u); }
DI float rbf(float x) { return bf_lo(pack_bf2(x, 0.f)); }

DI void glds16(const void* g, unsigned lds_addr) {
  unsigned keep;
  asm volatile("s_mov_b32 %0, m0\n\ts_mov_b32 m0, %2\n\ts_nop 0\n\tglobal_load_lds_dwordx4 %1, off\n\ts_mov_b32 m0, %0"
               : "=&s"(keep) : "v"(g), "s"(lds_addr) : "memory");
}
DI void glds16s(const void* sbase, unsigned voff, unsigned lds_addr) {
  unsigned keep;
  asm volatile("s_mov_b32 %0, m0\n\ts_mov_b32 m0, %3\n\ts_nop 0\n\tglobal_load_lds_dwordx4 %1, %2\n\ts_mov_b32 m0, %0"
               : "=&s"(keep) : "v"(voff), "s"(sbase), "s"(lds_addr) : "memory");
}
DI void wait_vm0() { asm volatile("s_waitcnt vmcnt(0)" ::: "memory"); }
DI void barrier_raw() {
  asm volatile("s_waitcnt lgkmcnt(0)" ::: "memory");
  __builtin_amdgcn_s_barrier();
  asm volatile("" ::: "memory");
}
template <int V> struct IC { static constexpr int value = V; };
template <int N, int I = 0, class F> DI void static_for(F&& f) {
  if constexpr (I < N) { f(IC<I>{}); static_for<N, I + 1>(f); }
}
DI s16x4 tr_read(const char* lds) {
  return __builtin_amdgcn_ds_read_tr16_b64_v4i16((lds_s16x4*)lds);
}

constexpr int G1_BOFF = 3 * 32768;
__global__ __launch_bounds__(512, 2) void gemm1_kernel(const u16* __restrict__ x, const u16* __restrict__ wqkv,
                                                       const u16* __restrict__ wg, const u16* __restrict__ wu,
                                                       u16* __restrict__ Qg, u16* __restrict__ Kf,
                                                       u16* __restrict__ Vf, u16* __restrict__ hbuf,
                                                       const u16* __restrict__ wd, const u16* __restrict__ wo,
                                                       u16* __restrict__ Bt, u16* __restrict__ Bf) {
  __shared__ __attribute__((aligned(16))) char smem[G1_BOFF + 2 * 24576];
  const int b = blockIdx.x;
  if (b >= 224) {
    const int tsp = (b - 224) * 512 + threadIdx.x;
    {
      u32x4 v[4];
#pragma unroll
      for (int u = 0; u < 4; ++u) {
        int it = tsp + u * 16384;
        if (it >= 640 * 80) it = 0;
        const int k = it / 80, c8 = it - k * 80;
        v[u] = *(const u32x4*)(wo + (size_t)k * DM + c8 * 8);
      }
#pragma unroll
      for (int u = 0; u < 4; ++u) {
        const int it = tsp + u * 16384;
        const int k = it / 80, c8 = it - k * 80;
        const int nb = c8 / 10, r10 = c8 - nb * 10, kb = k >> 3;
        u16* dst = Bt + ((size_t)(nb * 80 + kb) * 640 + (r10 >> 1) * 128 + ((k & 7) ^ ((kb & 1) << 2)) * 16 + (r10 & 1) * 8);
        if (it < 640 * 80) *(u32x4*)dst = v[u];
      }
    }
    for (int it = tsp; it < 216 * 640; it += 16384) {
      const int kb = it / 640, n = it - kb * 640;
      const u16* src = wd + (size_t)kb * 8 * DM + n;
      unsigned e[8];
#pragma unroll
      for (int j = 0; j < 8; ++j) e[j] = src[j * DM];
      const int nb = n / 80, r = n - nb * 80;
      u32x4 w = {e[0] | (e[1] << 16), e[2] | (e[3] << 16), e[4] | (e[5] << 16), e[6] | (e[7] << 16)};
      *(u32x4*)(Bf + ((size_t)((nb * 54 + (kb >> 2)) * 5 + (r >> 4)) * 64 + (kb & 3) * 16 + (r & 15)) * 8) = w;
    }
    return;
  }
  const int t = (b & 7) * 28 + (b >> 3);
  const int nblk = t >> 3, mblk = t & 7;
  const int tid = threadIdx.x, wave = tid >> 6, lane = tid & 63;
  const int wm = wave >> 1, wn = wave & 1;
  const int m0 = mblk * 256;
  const bool isqkv = nblk < 10;

  const unsigned a_vo = ((m0 + wave * 32 + (lane >> 3)) * DM + ((lane & 7) ^ (lane >> 4)) * 8) * 2;
  const int ldb = isqkv ? 3 * DM : DF;
  const u16* b_src[3];
#pragma unroll
  for (int i = 0; i < 3; ++i) {
    int kb = wave;
    int nt = i * 4 + (lane >> 4);
    int rr = ((lane >> 1) & 7) ^ ((kb & 1) << 2);
    int k = kb * 8 + rr;
    const u16* base;
    if (isqkv) base = wqkv + nblk * 192 + nt * 16;
    else base = (nt < 6) ? (wg + (nblk - 10) * 96 + nt * 16) : (wu + (nblk - 10) * 96 + (nt - 6) * 16);
    b_src[i] = base + (size_t)k * ldb + (lane & 1) * 8;
  }
  const unsigned lds0 = __builtin_amdgcn_readfirstlane((unsigned)(size_t)smem);
  const unsigned wv = __builtin_amdgcn_readfirstlane(wave);
  auto stage_a = [&](int kt, int g) {
    glds16s(x + g * 8 * DM + kt * 64, a_vo ^ ((g & 1) << 6), lds0 + (kt % 3) * 32768 + (wv * 4 + g) * 1024);
  };
  auto stage_b = [&](int kt, int g) {
    glds16(b_src[g] + (size_t)kt * 64 * ldb, lds0 + G1_BOFF + (kt & 1) * 24576 + (wv * 3 + g) * 1024);
  };
#pragma unroll
  for (int kt = 0; kt < 2; ++kt) {
#pragma unroll
    for (int g = 0; g < 4; ++g) stage_a(kt, g);
#pragma unroll
    for (int g = 0; g < 3; ++g) stage_b(kt, g);
  }

  const int a_off = ((wm * 64 + wn * 32 + (lane & 15)) * 128 + (((lane >> 4) ^ ((lane >> 1) & 7)) << 4)) ^ ((wm & 1) << 6);
  int b_off[2][2];
  {
    int g = lane >> 4, q = (lane & 15) >> 2, p = lane & 3;
    int base = G1_BOFF + g * 3072 + ((g & 1) << 7) + q * 32 + p * 8;
    int t0 = (isqkv ? wn * 6 : wn * 3) * 256, t1 = (isqkv ? wn * 6 + 3 : 6 + wn * 3) * 256;
    if (wm >= 2) { int tt = t0; t0 = t1; t1 = tt; }
#pragma unroll
    for (int kk = 0; kk < 2; ++kk) {
      b_off[kk][0] = base + t0 + (kk ^ (wm & 1)) * 12288;
      b_off[kk][1] = base + t1 + (kk ^ (wm & 1)) * 12288;
    }
  }

  f32x4 acc[4][6];
#pragma unroll
  for (int i = 0; i < 4; ++i)
#pragma unroll
    for (int j = 0; j < 6; ++j) acc[i][j] = (f32x4){0.f, 0.f, 0.f, 0.f};

  bf16x8 af0[4], bf0[6], af1[4], bf1[6];
  auto read_q = [&](bf16x8 (&af)[4], bf16x8 (&bfr)[6], int kt, int kk, int g) {
    const char* sa = smem + (kt % 3) * 32768;
    const char* sb = smem + (kt & 1) * 24576;
    auto rb = [&](int j) {
      s16x4 lo = tr_read(sb + b_off[kk][j / 3] + (j % 3) * 256);
      s16x4 hi = tr_read(sb + (b_off[kk][j / 3] ^ 128) + (j % 3) * 256);
      bfr[j] = __builtin_shufflevector(lo, hi, 0, 1, 2, 3, 4, 5, 6, 7);
    };
    auto ra = [&](int i) { af[i] = *(const bf16x8*)(sa + ((a_off ^ (kk << 6)) ^ ((i >> 1) << 12)) + (i & 1) * 2048); };
    if (g == 0) { ra(0); rb(0); rb(1); }
    if (g == 1) { rb(2); rb(3); }
    if (g == 2) { rb(4); rb(5); }
    if (g == 3) { ra(1); ra(2); ra(3); }
  };
  auto mma_q = [&](bf16x8 (&af)[4], bf16x8 (&bfr)[6], int i) {
#pragma unroll
    for (int j = 0; j < 6; ++j)
      acc[i][j] = __builtin_amdgcn_mfma_f32_16x16x32_bf16(bfr[j], af[i], acc[i][j], 0, 0, 0);
  };

  f32x4 rs[2], rq[2], cs[3];
#pragma unroll
  for (int j = 0; j < 2; ++j) { rs[j] = (f32x4){0.f, 0.f, 0.f, 0.f}; rq[j] = (f32x4){0.f, 0.f, 0.f, 0.f}; }
#pragma unroll
  for (int j = 0; j < 3; ++j) cs[j] = (f32x4){0.f, 0.f, 0.f, 0.f};
  const bf16x8 ones = {0x3F80, 0x3F80, 0x3F80, 0x3F80, 0x3F80, 0x3F80, 0x3F80, 0x3F80};
  auto stats_q = [&](bf16x8 (&af)[4], bf16x8 (&bfr)[6], int kk, int g) {
    if (g < 2) {
      rs[g] = __builtin_amdgcn_mfma_f32_16x16x32_bf16(ones, af[g], rs[g], 0, 0, 0);
      rq[g] = __builtin_amdgcn_mfma_f32_16x16x32_bf16(af[g], af[g], rq[g], 0, 0, 0);
    }
    if (g < 3 && kk == 0) cs[g] = __builtin_amdgcn_mfma_f32_16x16x32_bf16(bfr[g], ones, cs[g], 0, 0, 0);
  };

  asm volatile("s_waitcnt vmcnt(7)" ::: "memory");
  barrier_raw();
#pragma unroll
  for (int g = 0; g < 4; ++g) read_q(af0, bf0, 0, 0, g);
#pragma unroll
  for (int kt = 0; kt < 10; ++kt) {
#pragma unroll
    for (int g = 0; g < 4; ++g) {
      __builtin_amdgcn_sched_barrier(0);
      read_q(af1, bf1, kt, 1, g);
      mma_q(af0, bf0, g);
      stats_q(af0, bf0, 0, g);
      __builtin_amdgcn_sched_barrier(0);
      if (kt + 2 < 10) stage_a(kt + 2, g);
    }
    __builtin_amdgcn_sched_barrier(0);
    if (kt + 1 < 10) {
      if (kt + 2 < 10) asm volatile("s_waitcnt vmcnt(4)" ::: "memory");
      else wait_vm0();
      barrier_raw();
    }
#pragma unroll
    for (int g = 0; g < 4; ++g) {
      __builtin_amdgcn_sched_barrier(0);
      if (kt + 1 < 10) read_q(af0, bf0, kt + 1, 0, g);
      mma_q(af1, bf1, g);
      stats_q(af1, bf1, 1, g);
      __builtin_amdgcn_sched_barrier(0);
      if (kt + 2 < 10 && g < 3) stage_b(kt + 2, g);
    }
  }
  __builtin_amdgcn_sched_barrier(0);
  barrier_raw();

  int tz = threadIdx.x;
  asm volatile("" : "+v"(tz));
  const int e_wave = __builtin_amdgcn_readfirstlane(tz >> 6), e_lane = tz & 63, e_wm = e_wave >> 1, e_wn = e_wave & 1;
  const int ln4 = (e_lane >> 4) * 4;
  {
    f32x2* st_row = (f32x2*)(smem + 65536);
    float* st_col = (float*)(smem + 65536 + 4096);
#pragma unroll
    for (int i = 0; i < 2; ++i) {
      const int m = e_lane & 15;
      const float q = (m & 2) ? ((m & 1) ? rq[i][3] : rq[i][2]) : ((m & 1) ? rq[i][1] : rq[i][0]);
      if ((e_lane >> 4) == (m >> 2)) st_row[e_wm * 64 + e_wn * 32 + i * 16 + m] = (f32x2){rs[i][0], q};
    }
    if ((e_lane & 15) == 0) {
#pragma unroll
      for (int j = 0; j < 3; ++j)
        *(f32x4*)(st_col + ((e_wm & 1) * 2 + e_wn) * 96 + ((e_wm >> 1) * 3 + j) * 16 + ln4) = cs[j];
    }
    barrier_raw();
    float mb[4], inv[4];
#pragma unroll
    for (int i = 0; i < 4; ++i) {
      const f32x2 p0 = st_row[e_wm * 64 + ((i + 2 * e_wn) & 3) * 16 + (e_lane & 15)];
      const float mean = p0[0] * (1.f / DM);
      const float var = fmaxf(p0[1] * (1.f / DM) - mean * mean, 0.f);
      mb[i] = rbf(mean);
      inv[i] = __builtin_amdgcn_rcpf(rbf(__builtin_amdgcn_sqrtf(rbf(rbf(var) + 1e-5f))));
    }
#pragma unroll
    for (int j = 0; j < 6; ++j) {
      const int jl = (e_wm >= 2) ? (j + 3) % 6 : j;
      const f32x4 c = *(const f32x4*)(st_col + e_wn * 96 + jl * 16 + ln4) + *(const f32x4*)(st_col + (2 + e_wn) * 96 + jl * 16 + ln4);
#pragma unroll
      for (int i = 0; i < 4; ++i)
#pragma unroll
        for (int r = 0; r < 4; ++r) acc[i][j][r] = __builtin_fmaf(-mb[i], c[r], acc[i][j][r]) * inv[i];
    }
  }
  if (isqkv) {
    const float qscale = 0.22360679774997896f * 1.4426950408889634f;
    char* wtr = smem + e_wave * 512;
#pragma unroll
    for (int j = 0; j < 6; ++j) {
      const int nb = nblk * 192 + (e_wn * 6 + ((e_wm >= 2) ? (j + 3) % 6 : j)) * 16;
      const int sec = nb / DM;
      if (sec < 2) {
        const int rr = nb + ln4 - sec * DM, head = rr / DH, d = rr - head * DH, d4 = d >> 2;
#pragma unroll
        for (int i = 0; i < 4; ++i) {
          const int tok = m0 + e_wm * 64 + ((i + 2 * e_wn) & 3) * 16 + (e_lane & 15);
          f32x4 v = acc[i][j];
          if (sec == 0) {
            u32x2 o = {pack_bf2(v[0] * qscale, v[1] * qscale), pack_bf2(v[2] * qscale, v[3] * qscale)};
            *(u32x2*)(Qg + ((size_t)((tok >> 4) * 40 + (nb >> 4)) * 64 + e_lane) * 4) = o;
          } else {
            char* kb = (char*)Kf + (size_t)(head * 64 + (tok >> 5)) * 1536;
            const int r = tok & 31;
            unsigned p0 = pack_bf2(v[0], v[1]), p1 = pack_bf2(v[2], v[3]);
            if (d4 < 4) {
              u32x2 o = {p0, p1};
              *(u32x2*)(kb + ((d4 >> 1) * 32 + r) * 16 + (d4 & 1) * 8) = o;
            } else {
              u32x4 o = {p0, p1, 0x00003F80u, 0u};
              *(u32x4*)(kb + 1024 + r * 16) = o;
            }
          }
        }
      } else {
        const int rr = nb + (e_lane & 15) - 2 * DM, head = rr / DH, d = rr - head * DH;
        const int g = e_lane >> 4;
#pragma unroll
        for (int i = 0; i < 4; ++i) {
          f32x4 v = acc[i][j];
          u32x2 w = {pack_bf2(v[0], v[1]), pack_bf2(v[2], v[3])};
          *(u32x2*)(wtr + (e_lane & 15) * 32 + g * 8) = w;
          s16x4 tv = tr_read(wtr + g * 128 + ((e_lane & 15) >> 2) * 32 + (e_lane & 3) * 8);
          const int s = (m0 + e_wm * 64 + ((i + 2 * e_wn) & 3) * 16) >> 4;
          u16* vb = Vf + ((size_t)(head * 128 + s) * 48 + (g & 1) * 24 + d) * 8 + (g >> 1) * 4;
          *(s16x4*)vb = tv;
          if (d == DH - 1) { s16x4 one = {0x3F80, 0x3F80, 0x3F80, 0x3F80}; *(s16x4*)(vb + 8) = one; }
        }
      }
    }
  } else {
#pragma unroll
    for (int j = 0; j < 3; ++j) {
      const int f = (nblk - 10) * 96 + (e_wn * 3 + j) * 16 + ln4;
#pragma unroll
      for (int i = 0; i < 4; ++i) {
        const int tok = m0 + e_wm * 64 + ((i + 2 * e_wn) & 3) * 16 + (e_lane & 15);
        float hv[4];
#pragma unroll
        for (int r = 0; r < 4; ++r) {
          const float g = (e_wm >= 2) ? acc[i][j + 3][r] : acc[i][j][r], u = (e_wm >= 2) ? acc[i][j][r] : acc[i][j + 3][r];
          const float e = __builtin_amdgcn_exp2f(g * __builtin_fmaf(g * g, -0.10294324f, -2.30220819f));
          hv[r] = (g * u) * __builtin_amdgcn_rcpf(1.f + e);
        }
        u32x2 o = {pack_bf2(hv[0], hv[1]), pack_bf2(hv[2], hv[3])};
        *(u32x2*)(hbuf + ((size_t)((tok >> 4) * 54 + (f >> 5)) * 64 + ((f >> 3) & 3) * 16 + (tok & 15)) * 8 + (f & 7)) = o;
      }
    }
  }
}

constexpr int AT_STAGE = 24576;
#define AT_BIAS_V 8.f
constexpr float AT_BIAS = AT_BIAS_V;
#define AT_LIMIT_V 1073741824.f
constexpr float AT_LIMIT = AT_LIMIT_V;
#define MFMA32(a, b, c) __builtin_amdgcn_mfma_f32_32x32x16_bf16((a), (b), (c), 0, 0, 0)
#define MFMA16(a, b, c) __builtin_amdgcn_mfma_f32_16x16x32_bf16((a), (b), (c), 0, 0, 0)

DI void attn_role(char* smem, int b, const u16* __restrict__ Qg, const u16* __restrict__ Kf,
                  const u16* __restrict__ Vf, u16* __restrict__ obuf) {
  const int head = (b & 7) * 4 + ((b >> 3) & 3), qb = b >> 5;
  const int tid = threadIdx.x, wave = tid >> 6, lane = tid & 63;
  const int r = lane & 31, h = lane >> 5;
  const int q = qb * 256 + wave * 32 + r;

  const unsigned lds0 = __builtin_amdgcn_readfirstlane((unsigned)(size_t)smem);
  const unsigned wv = __builtin_amdgcn_readfirstlane(wave);
  const char* gsrc = ((wv < 4) ? (const char*)Kf + wv * 3072 : (const char*)Vf + (wv - 4) * 3072) +
                     (size_t)head * 98304 + lane * 16;
  auto stage = [&](unsigned boff, int tile) {
#pragma unroll
    for (int i = 0; i < 3; ++i) glds16(gsrc + tile * 12288 + i * 1024, lds0 + boff + (wv * 3 + i) * 1024);
  };
  auto qload = [&](int d) {
    const int col = head * DH + d;
    return *(const u32x2*)(Qg + ((size_t)((q >> 4) * 40 + (col >> 4)) * 64 + ((col >> 2) & 3) * 16 + (q & 15)) * 4);
  };
  bf16x8 q1;
  u32x4 q2 = {0u, 0u, 0u, 0u};
  {
    u32x2 ta = qload(8 * h), tb = qload(8 * h + 4), t2 = qload(16);
    u32x4 t = {ta[0], ta[1], tb[0], tb[1]};
    q1 = __builtin_bit_cast(bf16x8, t);
    if (h == 0) { q2[0] = t2[0]; q2[1] = t2[1]; }
  }
  stage(0, 0);
  stage(AT_STAGE, 1);

  const int k1_off = lane * 16, k2_off = 1024 + r * 16;
  const int v_off = 12288 + (h * 24 + (r < 23 ? r : 23)) * 16;

  f32x16 o, s0, s1;
  const f32x16 zero = {0.f, 0.f, 0.f, 0.f, 0.f, 0.f, 0.f, 0.f, 0.f, 0.f, 0.f, 0.f, 0.f, 0.f, 0.f, 0.f};
  o = zero;
  float mref;

  asm volatile("s_waitcnt vmcnt(3)" ::: "memory");
  barrier_raw();
  {
    s0 = MFMA32(*(const bf16x8*)(smem + k1_off), q1, zero);
    s0 = MFMA32(*(const bf16x8*)(smem + k2_off), __builtin_bit_cast(bf16x8, q2), s0);
    s1 = MFMA32(*(const bf16x8*)(smem + 1536 + k1_off), q1, zero);
    s1 = MFMA32(*(const bf16x8*)(smem + 1536 + k2_off), __builtin_bit_cast(bf16x8, q2), s1);
    float mx = fmaxf(s0[0], s1[0]);
#pragma unroll
    for (int i = 1; i < 16; ++i) mx = fmaxf(mx, fmaxf(s0[i], s1[i]));
    mref = rbf(fmaxf(mx, __shfl_xor(mx, 32)) + AT_BIAS);
    if (h == 0) q2[2] = pack_bf2(-mref, 0.f);
#pragma unroll
    for (int i = 0; i < 16; ++i) { s0[i] -= mref; s1[i] -= mref; }
  }
  unsigned cur = 0;
  for (int tile = 0; tile < 8; ++tile) {
    const unsigned nxt = (cur == 2 * AT_STAGE) ? 0 : cur + AT_STAGE;
    float osave[12];
#pragma unroll
    for (int i = 0; i < 12; ++i) osave[i] = o[i];
#pragma unroll
    for (int g = 0; g < 4; ++g) {
      f32x16 n0, n1;
      const char* kb;
      if (g == 3) {
        if (tile < 7) {
          wait_vm0();
          barrier_raw();
          if (tile < 6) stage((nxt == 2 * AT_STAGE) ? 0 : nxt + AT_STAGE, tile + 2);
        }
        kb = smem + nxt;
      } else {
        kb = smem + cur + (g + 1) * 3072;
      }
      n0 = MFMA32(*(const bf16x8*)(kb + k1_off), q1, zero);
      n0 = MFMA32(*(const bf16x8*)(kb + k2_off), __builtin_bit_cast(bf16x8, q2), n0);
      n1 = MFMA32(*(const bf16x8*)(kb + 1536 + k1_off), q1, zero);
      n1 = MFMA32(*(const bf16x8*)(kb + 1536 + k2_off), __builtin_bit_cast(bf16x8, q2), n1);
#pragma unroll
      for (int s = 0; s < 4; ++s) {
        const f32x16& sv = (s < 2) ? s0 : s1;
        u32x4 pv;
#pragma unroll
        for (int i = 0; i < 4; ++i)
          pv[i] = pack_bf2(__builtin_amdgcn_exp2f(sv[(s & 1) * 8 + 2 * i]), __builtin_amdgcn_exp2f(sv[(s & 1) * 8 + 2 * i + 1]));
        bf16x8 vf = *(const bf16x8*)(smem + cur + (g * 4 + s) * 768 + v_off);
        o = MFMA32(vf, __builtin_bit_cast(bf16x8, pv), o);
      }
      s0 = n0; s1 = n1;
      __builtin_amdgcn_sched_group_barrier(0x100, 6, 0);
#pragma unroll
      for (int k = 0; k < 4; ++k) {
        __builtin_amdgcn_sched_group_barrier(0x400, 4, 0);
        __builtin_amdgcn_sched_group_barrier(0x008, 1, 0);
      }
      __builtin_amdgcn_sched_barrier(0);
    }
    if (__builtin_amdgcn_ballot_w64(h == 1 && !(o[8] < AT_LIMIT)) != 0) {
      float mx = -3.0e38f;
#pragma unroll 1
      for (int hf = 0; hf < 8; ++hf) {
        const char* kc = smem + cur + hf * 1536;
        f32x16 t = MFMA32(*(const bf16x8*)(kc + k1_off), q1, zero);
        t = MFMA32(*(const bf16x8*)(kc + k2_off), __builtin_bit_cast(bf16x8, q2), t);
#pragma unroll
        for (int i = 0; i < 16; ++i) mx = fmaxf(mx, t[i]);
      }
      mx = fmaxf(mx, __shfl_xor(mx, 32));
      const float mnew = rbf(mref + fmaxf(mx + AT_BIAS, 0.f));
      const float delta = mnew - mref;
      const float alpha = __builtin_amdgcn_exp2f(-delta);
      mref = mnew;
      if (h == 0) q2[2] = pack_bf2(-mref, 0.f);
#pragma unroll
      for (int i = 0; i < 16; ++i) { s0[i] -= delta; s1[i] -= delta; o[i] = (i < 12) ? osave[i] * alpha : 0.f; }
#pragma unroll 1
      for (int hf = 0; hf < 8; ++hf) {
        const char* kc = smem + cur + hf * 1536;
        f32x16 t = MFMA32(*(const bf16x8*)(kc + k1_off), q1, zero);
        t = MFMA32(*(const bf16x8*)(kc + k2_off), __builtin_bit_cast(bf16x8, q2), t);
#pragma unroll
        for (int s = 0; s < 2; ++s) {
          u32x4 pv;
#pragma unroll
          for (int i = 0; i < 4; ++i)
            pv[i] = pack_bf2(__builtin_amdgcn_exp2f(t[s * 8 + 2 * i]), __builtin_amdgcn_exp2f(t[s * 8 + 2 * i + 1]));
          bf16x8 vf = *(const bf16x8*)(smem + cur + (hf * 2 + s) * 768 + v_off);
          o = MFMA32(vf, __builtin_bit_cast(bf16x8, pv), o);
        }
      }
    }
    cur = nxt;
  }
  const float l = __shfl(o[8], r + 32);
  const float inv = 1.f / l;
  auto ostore = [&](int d, u32x2 w) {
    const int col = head * DH + d;
    *(u32x2*)(obuf + ((size_t)((q >> 4) * 20 + (col >> 5)) * 64 + ((col >> 3) & 3) * 16 + (q & 15)) * 8 + (col & 7)) = w;
  };
  {
    u32x2 w0 = {pack_bf2(o[0] * inv, o[1] * inv), pack_bf2(o[2] * inv, o[3] * inv)};
    u32x2 w1 = {pack_bf2(o[4] * inv, o[5] * inv), pack_bf2(o[6] * inv, o[7] * inv)};
    ostore(4 * h, w0);
    ostore(4 * h + 8, w1);
    if (h == 0) {
      u32x2 w2 = {pack_bf2(o[8] * inv, o[9] * inv), pack_bf2(o[10] * inv, o[11] * inv)};
      ostore(16, w2);
    }
  }
}

DI void down_role(char* smem, int b, const u16* __restrict__ hbuf, const u16* __restrict__ Bf,
                  const u16* __restrict__ x, float* __restrict__ y) {
  const int t = (b & 7) * 32 + (b >> 3);
  const int nblk = t & 7, mblk = t >> 3;
  const int tid = threadIdx.x, wave = tid >> 6, lane = tid & 63;
  const int m0 = mblk * 64, n0 = nblk * 80;
  const unsigned wv = __builtin_amdgcn_readfirstlane(wave);
  const int mh = wave & 1, ks = wave >> 1;
  const u16* ap = hbuf + ((size_t)((mblk * 4 + mh * 2) * 54 + ks) * 64 + lane) * 8;
  const u16* bp = Bf + ((size_t)((nblk * 54 + ks) * 5) * 64 + lane) * 8;
  f32x4 acc[2][5];
#pragma unroll
  for (int i = 0; i < 2; ++i)
#pragma unroll
    for (int j = 0; j < 5; ++j) acc[i][j] = (f32x4){0.f, 0.f, 0.f, 0.f};
  bf16x8 a0[2], b0[5], a1[2], b1[5];
  auto load = [&](bf16x8 (&af)[2], bf16x8 (&bfr)[5], int it) {
    af[0] = *(const bf16x8*)(ap + it * 4 * 512);
    af[1] = *(const bf16x8*)(ap + 54 * 512 + it * 4 * 512);
#pragma unroll
    for (int j = 0; j < 5; ++j) bfr[j] = *(const bf16x8*)(bp + (it * 20 + j) * 512);
  };
  auto mma = [&](bf16x8 (&af)[2], bf16x8 (&bfr)[5]) {
#pragma unroll
    for (int j = 0; j < 5; ++j) {
      acc[0][j] = MFMA16(bfr[j], af[0], acc[0][j]);
      acc[1][j] = MFMA16(bfr[j], af[1], acc[1][j]);
    }
  };
  load(a0, b0, 0);
  for (int it = 0; it < 12; it += 2) {
    load(a1, b1, it + 1);
    mma(a0, b0);
    load(a0, b0, it + 2);
    mma(a1, b1);
  }
  if (wv < 4) load(a1, b1, 13);
  mma(a0, b0);
  if (wv < 4) mma(a1, b1);
  if (wv >= 2) {
    f32x4* red = (f32x4*)smem + (wave - 2) * 640 + lane;
#pragma unroll
    for (int i = 0; i < 2; ++i)
#pragma unroll
      for (int j = 0; j < 5; ++j) red[(i * 5 + j) * 64] = acc[i][j];
  }
  barrier_raw();
  if (wv < 2) {
#pragma unroll
    for (int i = 0; i < 2; ++i) {
      const size_t gi = (size_t)(m0 + mh * 32 + i * 16 + (lane & 15)) * DM + n0 + (lane >> 4) * 4;
#pragma unroll
      for (int j = 0; j < 5; ++j) {
        f32x4 s = acc[i][j];
#pragma unroll
        for (int w = 0; w < 3; ++w) s += ((const f32x4*)smem)[(w * 2 + mh) * 640 + (i * 5 + j) * 64 + lane];
        u32x2 xv = *(const u32x2*)(x + gi + j * 16);
        s[0] += bf_lo(xv[0]); s[1] += bf_hi(xv[0]); s[2] += bf_lo(xv[1]); s[3] += bf_hi(xv[1]);
        *(f32x4*)(y + ((size_t)(t * 20 + mh * 10 + i * 5 + j) * 64 + lane) * 4) = s;
      }
    }
  }
}

__global__ __launch_bounds__(512, 4) void k2_kernel(const u16* __restrict__ Qg, const u16* __restrict__ Kf,
                                                    const u16* __restrict__ Vf, u16* __restrict__ obuf,
                                                    const u16* __restrict__ hbuf, const u16* __restrict__ Bf,
                                                    const u16* __restrict__ x, float* __restrict__ y) {
  __shared__ __attribute__((aligned(16))) char smem[3 * AT_STAGE];
  const int b = blockIdx.x;
  if (b < 256) attn_role(smem, b, Qg, Kf, Vf, obuf);
  else down_role(smem, b - 256, hbuf, Bf, x, y);
}

__global__ __launch_bounds__(512, 2) void k3_kernel(const u16* __restrict__ obuf, const u16* __restrict__ Bt,
                                                    const float* __restrict__ y, u16* __restrict__ out) {
  __shared__ __attribute__((aligned(16))) char smem[80 * 1280];
  const int b = blockIdx.x;
  const int t = (b & 7) * 32 + (b >> 3);
  const int nblk = t & 7, mblk = t >> 3;
  const int tid = threadIdx.x, wave = tid >> 6, lane = tid & 63;
  const int m0 = mblk * 64, n0 = nblk * 80;
  const unsigned lds0 = __builtin_amdgcn_readfirstlane((unsigned)(size_t)smem);
  const unsigned wv = __builtin_amdgcn_readfirstlane(wave);
  const int mh = wave & 1, ks = wave >> 1;
  const unsigned b_vo = nblk * 80 * 1280 + wave * 1024 + lane * 16;
  const u16* ap = obuf + ((size_t)((mblk * 4 + mh * 2) * 20 + ks) * 64 + lane) * 8;
  bf16x8 af[5][2];
  asm volatile("global_load_dwordx4 %0, %4, off offset:-2048\n\tglobal_load_dwordx4 %1, %5, off offset:-2048\n\t"
               "global_load_dwordx4 %2, %4, off offset:2048\n\tglobal_load_dwordx4 %3, %5, off offset:2048"
               : "=&v"(af[0][0]), "=&v"(af[0][1]), "=&v"(af[1][0]), "=&v"(af[1][1])
               : "v"(ap + 1024), "v"(ap + 20 * 512 + 1024) : "memory");
#pragma unroll
  for (int i = 0; i < 5; ++i) glds16s(Bt + i * 4096, b_vo, lds0 + (i * 8 + wv) * 1024);
  asm volatile("global_load_dwordx4 %0, %6, off offset:-2048\n\tglobal_load_dwordx4 %1, %7, off offset:-2048\n\t"
               "global_load_dwordx4 %2, %6, off offset:2048\n\tglobal_load_dwordx4 %3, %7, off offset:2048\n\t"
               "global_load_dwordx4 %4, %8, off\n\tglobal_load_dwordx4 %5, %9, off"
               : "=&v"(af[2][0]), "=&v"(af[2][1]), "=&v"(af[3][0]), "=&v"(af[3][1]), "=&v"(af[4][0]), "=&v"(af[4][1])
               : "v"(ap + 2 * 2048 + 1024), "v"(ap + 20 * 512 + 2 * 2048 + 1024), "v"(ap + 4 * 2048), "v"(ap + 20 * 512 + 4 * 2048)
               : "memory");
#pragma unroll
  for (int i = 5; i < 13; ++i)
    glds16s(Bt + ((i < 12 || wv < 4) ? i : 11) * 4096, b_vo, lds0 + (((i < 12 || wv < 4) ? i : 11) * 8 + wv) * 1024);
  f32x4 yv[3];
  {
    const float* yp = y + ((size_t)(t * 20 + wave) * 64 + lane) * 4;
    asm volatile("global_load_dwordx4 %0, %3, off\n\tglobal_load_dwordx4 %1, %4, off\n\tglobal_load_dwordx4 %2, %5, off"
                 : "=&v"(yv[0]), "=&v"(yv[1]), "=&v"(yv[2])
                 : "v"(yp), "v"(yp + 8 * 256), "v"(yp + (wv < 4 ? 16 : 8) * 256) : "memory");
  }
  f32x4 acc[2][5];
#pragma unroll
  for (int i = 0; i < 2; ++i)
#pragma unroll
    for (int j = 0; j < 5; ++j) acc[i][j] = (f32x4){0.f, 0.f, 0.f, 0.f};
  int b_off;
  {
    int g = lane >> 4, q = (lane & 15) >> 2, p = lane & 3;
    b_off = (ks * 4 + g) * 1280 + ((g & 1) << 7) + q * 32 + p * 8;
  }
  auto steps = [&](int s0, int s1) {
#pragma unroll
    for (int s = 0; s < 5; ++s) {
      if (s < s0 || s >= s1) continue;
      bf16x8 bfr[5];
#pragma unroll
      for (int j = 0; j < 5; ++j) {
        s16x4 lo = tr_read(smem + b_off + s * 20480 + j * 256);
        s16x4 hi = tr_read(smem + (b_off ^ 128) + s * 20480 + j * 256);
        bfr[j] = __builtin_shufflevector(lo, hi, 0, 1, 2, 3, 4, 5, 6, 7);
      }
#pragma unroll
      for (int j = 0; j < 5; ++j) {
        acc[0][j] = MFMA16(bfr[j], af[s][0], acc[0][j]);
        acc[1][j] = MFMA16(bfr[j], af[s][1], acc[1][j]);
      }
    }
  };
  asm volatile("s_waitcnt vmcnt(17)" : "+v"(af[0][0]), "+v"(af[0][1]), "+v"(af[1][0]), "+v"(af[1][1]) :: "memory");
  barrier_raw();
  steps(0, 2);
  asm volatile("s_waitcnt vmcnt(0)"
               : "+v"(af[2][0]), "+v"(af[2][1]), "+v"(af[3][0]), "+v"(af[3][1]), "+v"(af[4][0]), "+v"(af[4][1]),
                 "+v"(yv[0]), "+v"(yv[1]), "+v"(yv[2]) :: "memory");
  barrier_raw();
  steps(2, 5);
  barrier_raw();
  {
    f32x4* red = (f32x4*)smem + (ks * 20 + mh * 10) * 64 + lane;
#pragma unroll
    for (int i = 0; i < 2; ++i)
#pragma unroll
      for (int j = 0; j < 5; ++j) red[(i * 5 + j) * 64] = acc[i][j];
  }
  barrier_raw();
#pragma unroll
  for (int u = 0; u < 3; ++u) {
    const int tile = wave + u * 8;
    if (u < 2 || wv < 4) {
      f32x4 s = yv[u];
#pragma unroll
      for (int w = 0; w < 4; ++w) s += ((const f32x4*)smem)[(w * 20 + tile) * 64 + lane];
      const int rt = tile / 5, j = tile - rt * 5;
      u32x2 o = {pack_bf2(s[0], s[1]), pack_bf2(s[2], s[3])};
      *(u32x2*)(out + (size_t)(m0 + rt * 16 + (lane & 15)) * DM + n0 + j * 16 + (lane >> 4) * 4) = o;
    }
  }
}

extern "C" void kernel_launch(void* const* d_in, const int* in_sizes, int n_in, void* d_out, int out_size,
                              void* d_ws, size_t ws_size, hipStream_t stream) {
  const u16* x = (const u16*)d_in[0];
  const u16* wqkv = (const u16*)d_in[1];
  const u16* wo = (const u16*)d_in[2];
  const u16* wg = (const u16*)d_in[4];
  const u16* wu = (const u16*)d_in[5];
  const u16* wd = (const u16*)d_in[6];
  char* ws = (char*)d_ws;
  u16* Qg = (u16*)(ws + (4u << 20));
  u16* Kf = (u16*)(ws + (8u << 20));
  u16* Vf = (u16*)(ws + (12u << 20));
  u16* hb = (u16*)(ws + (16u << 20));
  u16* ob = (u16*)(ws + (24u << 20));
  float* yb = (float*)(ws + (28u << 20));
  u16* Bt = (u16*)(ws + (34u << 20));
  u16* Bf = (u16*)(ws + (35u << 20));
  gemm1_kernel<<<256, 512, 0, stream>>>(x, wqkv, wg, wu, Qg, Kf, Vf, hb, wd, wo, Bt, Bf);
  k2_kernel<<<512, 512, 0, stream>>>(Qg, Kf, Vf, ob, hb, Bf, x, yb);
  k3_kernel<<<256, 512, 0, stream>>>(ob, Bt, yb, (u16*)d_out);
}
```
